```python
import torch

def _softmax(x, axis=-1):
    return torch.softmax(x, dim=axis)

def _gelu(x):
    return torch.nn.functional.gelu(x, approximate='tanh')

def _silu(x):
    return torch.nn.functional.silu(x)

def _sigmoid(x):
    return torch.sigmoid(x)

def _layernorm(x, axis=-1, eps=1e-05):
    m = x.mean(dim=axis, keepdim=True)
    v = x.var(dim=axis, keepdim=True, unbiased=False)
    return (x - m) / torch.sqrt(v + eps)

def _scatter_add(tgt, idx, src, axis=0):
    return tgt.clone().index_add_(axis, idx, src)

def _segment_sum(src, seg, n):
    out = torch.zeros((n,) + src.shape[1:], dtype=src.dtype)
    return out.index_add_(0, seg, src)

def _pad(x, pw):
    flat = tuple((v for pair in reversed(pw) for v in pair))
    return torch.nn.functional.pad(x, flat)
kernel_name = 'adaLN_cond__spectral_gfnet__moe_arch_mod__prompt'

def setup_inputs():

    _w = lambda a: (lambda t: t.to(torch.bfloat16) if t.is_floating_point() else t)(torch.as_tensor(a))

    def _A_setup():
        B, S, D = (1, 2048, 640)
        Df = D // 2 + 1
        return {'x': _w((lambda *s: torch.randn(*s, dtype=torch.bfloat16) * 0.05)(B, S, D)), 'w_r': _w((lambda *s: torch.randn(*s, dtype=torch.bfloat16) * 3.2)(S, Df) * 0.02), 'w_i': _w((lambda *s: torch.randn(*s, dtype=torch.bfloat16) * 3.2)(S, Df) * 0.02)}
    _A_in = _A_setup()

    def _B_setup():
        B, S, D, F, C = (1, 2048, 640, 1536, 1)
        return {'x': _w((lambda *s: torch.randn(*s, dtype=torch.bfloat16) * 0.05)(B, S, D)), 'w_r': _w((lambda *s: torch.randn(*s, dtype=torch.bfloat16) * 0.05)(D)), 'w_gate': _w((lambda *s: torch.randn(*s, dtype=torch.bfloat16) * 0.05)(D, F)), 'w_up': _w((lambda *s: torch.randn(*s, dtype=torch.bfloat16) * 0.05)(D, F)), 'w_down': _w((lambda *s: torch.randn(*s, dtype=torch.bfloat16) * 0.05)(F, D))}
    _B_in = _B_setup()
    return {'A_x': _A_in['x'], 'A_w_r': _A_in['w_r'], 'A_w_i': _A_in['w_i'], 'B_x': _B_in['x'], 'B_w_r': _B_in['w_r'], 'B_w_gate': _B_in['w_gate'], 'B_w_up': _B_in['w_up'], 'B_w_down': _B_in['w_down'], 'c': _w((lambda *s: torch.randn(*s, dtype=torch.bfloat16) * 0.1)(1, 640) * 8.0), 'w_mod': _w((lambda *s: torch.randn(*s, dtype=torch.bfloat16) * 0.1)(640, 6 * 640) * 640 ** (-0.5) * 8.0)}

def reference(A_x, A_w_r, A_w_i, B_x, B_w_r, B_w_gate, B_w_up, B_w_down, c, w_mod):

    def _A_ref(x, w_r, w_i):
        B, S, D = x.shape
        X = (lambda x, axes=(-2, -1): torch.fft.rfft2(x.float(), dim=axes))(x, axes=(-2, -1))
        W = w_r + 1j * w_i
        X = X * W
        y = (lambda x, s, axes=(-2, -1): torch.fft.irfft2(x, s=s, dim=axes).to(torch.bfloat16))(X, (S, D), axes=(-2, -1))
        return y

    def _B_ref(x, w_r, w_gate, w_up, w_down):
        B, S, D = x.shape
        C = 1
        r = x @ w_r
        idx = torch.argsort(-r, axis=-1)[:, :C]
        idx_e = (lambda x, n, axis: torch.repeat_interleave(x, n, dim=axis))(idx.reshape(B, C, 1), D, axis=2)
        x_sel = (lambda x, i, axis: torch.take_along_dim(x, i, dim=axis))(x, idx_e, axis=1)
        g = _silu(x_sel @ w_gate)
        u = x_sel @ w_up
        y = g * u @ w_down
        r_sel = _sigmoid((lambda x, i, axis: torch.take_along_dim(x, i, dim=axis))(r, idx, axis=1))
        y = y * r_sel.reshape(B, C, 1)
        b_off = (torch.arange(B) * S).reshape(B, 1)
        flat_idx = (idx + b_off).reshape(B * C)
        zero = (x * 0.0).reshape(B * S, D)
        upd = _scatter_add(zero, flat_idx, y.reshape(B * C, D), axis=0)
        return x + upd.reshape(B, S, D)
    x = A_x
    mod = (_silu(c) @ w_mod).reshape(c.shape[0], 6, 1, -1)
    sa, ca, ga, sb, cb, gb = (mod[:, 0].clone(), mod[:, 1].clone(), mod[:, 2].clone(), mod[:, 3].clone(), mod[:, 4].clone(), mod[:, 5].clone())
    h = _layernorm(x, axis=-1) * (1 + ca) + sa
    x = x + ga * _A_ref(h, A_w_r, A_w_i)
    h = _layernorm(x, axis=-1) * (1 + cb) + sb
    x = x + gb * _B_ref(h, B_w_r, B_w_gate, B_w_up, B_w_down)
    return x
```

```cpp
#include <hip/hip_runtime.h>
#include <stdint.h>

typedef unsigned short bf16_t;
constexpr int S = 2048, D = 640, DF = 321, F = 1536;

__device__ __forceinline__ float bf2f(bf16_t v) { return __uint_as_float(((unsigned)v) << 16); }
__device__ __forceinline__ bf16_t f2bf(float f) { unsigned u = __float_as_uint(f); return (bf16_t)((u + 0x7fffu + ((u >> 16) & 1u)) >> 16); }
__device__ __forceinline__ float wsum(float v) { for (int o = 32; o; o >>= 1) v += __shfl_xor(v, o); return v; }

__global__ void k_mod(const bf16_t* c, const bf16_t* wmod, float* mod) {
    __shared__ float sc[D];
    for (int i = threadIdx.x; i < D; i += blockDim.x) { float v = bf2f(c[i]); sc[i] = v / (1.f + __expf(-v)); }
    __syncthreads();
    int j = blockIdx.x * 64 + (threadIdx.x & 63), w = threadIdx.x >> 6;
    float a = 0;
    for (int i = w * 160; i < w * 160 + 160; ++i) a += sc[i] * bf2f(wmod[(size_t)i * 6 * D + j]);
    __shared__ float red[4][64];
    red[w][threadIdx.x & 63] = a;
    __syncthreads();
    if (w == 0) mod[j] = red[0][threadIdx.x] + red[1][threadIdx.x] + red[2][threadIdx.x] + red[3][threadIdx.x];
}

__device__ float block_sum(float v, float* red) {
    v = wsum(v);
    __syncthreads();
    if ((threadIdx.x & 63) == 0) red[threadIdx.x >> 6] = v;
    __syncthreads();
    float t = 0;
    for (int i = 0; i < (int)(blockDim.x >> 6); ++i) t += red[i];
    return t;
}

__global__ void k_row1(const bf16_t* x, const float* mod, float2* X1) {
    __shared__ float h[D], ct[D], st[D], red[8];
    int s = blockIdx.x, t = threadIdx.x;
    float v0 = bf2f(x[(size_t)s * D + t]), v1 = bf2f(x[(size_t)s * D + t + 320]);
    float m = block_sum(v0 + v1, red) / D;
    float var = block_sum((v0 - m) * (v0 - m) + (v1 - m) * (v1 - m), red) / D;
    float rs = rsqrtf(var + 1e-5f);
    h[t] = (v0 - m) * rs * (1.f + mod[D + t]) + mod[t];
    h[t + 320] = (v1 - m) * rs * (1.f + mod[D + t + 320]) + mod[t + 320];
    for (int i = t; i < D; i += 320) { float sn, cs; sincospif(2.f * i / D, &sn, &cs); ct[i] = cs; st[i] = sn; }
    __syncthreads();
    for (int k = t; k < DF; k += 320) {
        float re = 0, im = 0; int idx = 0;
        for (int d = 0; d < D; ++d) { re += h[d] * ct[idx]; im -= h[d] * st[idx]; idx += k; if (idx >= D) idx -= D; }
        X1[(size_t)k * S + s] = make_float2(re, im);
    }
}

__device__ void fft2048(float2* a, float2* b, const float2* tw, float sgn) {
    float2 *src = a, *dst = b;
    for (int Ns = 1; Ns < S; Ns <<= 1) {
        for (int j = threadIdx.x; j < S / 2; j += blockDim.x) {
            int k = j & (Ns - 1);
            float2 w = tw[k * (S / (2 * Ns))]; w.y *= sgn;
            float2 u = src[j], v = src[j + S / 2];
            float2 vw = make_float2(v.x * w.x - v.y * w.y, v.x * w.y + v.y * w.x);
            int o = (j - k) * 2 + k;
            dst[o] = make_float2(u.x + vw.x, u.y + vw.y);
            dst[o + Ns] = make_float2(u.x - vw.x, u.y - vw.y);
        }
        __syncthreads();
        float2* t = src; src = dst; dst = t;
    }
}

__global__ void k_col(float2* X1, const bf16_t* wr, const bf16_t* wi) {
    __shared__ float2 a[S], b[S], tw[S / 2];
    int k = blockIdx.x;
    for (int i = threadIdx.x; i < S; i += blockDim.x) a[i] = X1[(size_t)k * S + i];
    for (int i = threadIdx.x; i < S / 2; i += blockDim.x) { float sn, cs; sincospif(2.f * i / S, &sn, &cs); tw[i] = make_float2(cs, sn); }
    __syncthreads();
    fft2048(a, b, tw, -1.f);
    for (int i = threadIdx.x; i < S; i += blockDim.x) {
        float2 v = b[i]; float r = bf2f(wr[(size_t)i * DF + k]), q = bf2f(wi[(size_t)i * DF + k]);
        b[i] = make_float2(v.x * r - v.y * q, v.x * q + v.y * r);
    }
    __syncthreads();
    fft2048(b, a, tw, 1.f);
    for (int i = threadIdx.x; i < S; i += blockDim.x) X1[(size_t)k * S + i] = a[i];
}

__global__ void k_row2(const bf16_t* x, const float* mod, const float2* X1, const bf16_t* wr, bf16_t* out, float* h2, float* x2, float* r) {
    __shared__ float2 z[DF];
    __shared__ float ct[D], st[D], red[8];
    int s = blockIdx.x, t = threadIdx.x;
    for (int k = t; k < DF; k += 320) z[k] = X1[(size_t)k * S + s];
    for (int i = t; i < D; i += 320) { float sn, cs; sincospif(2.f * i / D, &sn, &cs); ct[i] = cs; st[i] = sn; }
    __syncthreads();
    float xv[2];
    for (int q = 0; q < 2; ++q) {
        int d = t + 320 * q;
        float a = 0; int idx = d;
        for (int k = 1; k < 320; ++k) { a += z[k].x * ct[idx] - z[k].y * st[idx]; idx += d; if (idx >= D) idx -= D; }
        a = 2.f * a + z[0].x + ((d & 1) ? -z[320].x : z[320].x);
        a *= 1.f / (S * D);
        xv[q] = bf2f(x[(size_t)s * D + d]) + mod[2 * D + d] * a;
    }
    float m = block_sum(xv[0] + xv[1], red) / D;
    float var = block_sum((xv[0] - m) * (xv[0] - m) + (xv[1] - m) * (xv[1] - m), red) / D;
    float rs = rsqrtf(var + 1e-5f), rp = 0;
    for (int q = 0; q < 2; ++q) {
        int d = t + 320 * q;
        float h = (xv[q] - m) * rs * (1.f + mod[4 * D + d]) + mod[3 * D + d];
        rp += h * bf2f(wr[d]);
        h2[(size_t)s * D + d] = h; x2[(size_t)s * D + d] = xv[q];
        out[(size_t)s * D + d] = f2bf(xv[q] + mod[5 * D + d] * h);
    }
    rp = block_sum(rp, red);
    if (t == 0) r[s] = rp;
}

__device__ int argmax_r(const float* r, float* bv, int* bi) {
    float best = -INFINITY; int idx = 0;
    for (int i = threadIdx.x; i < S; i += blockDim.x) { float v = r[i]; if (v > best) { best = v; idx = i; } }
    bv[threadIdx.x] = best; bi[threadIdx.x] = idx;
    __syncthreads();
    for (int o = blockDim.x / 2; o; o >>= 1) {
        if (threadIdx.x < o) { float v = bv[threadIdx.x + o]; int j = bi[threadIdx.x + o];
            if (v > bv[threadIdx.x] || (v == bv[threadIdx.x] && j < bi[threadIdx.x])) { bv[threadIdx.x] = v; bi[threadIdx.x] = j; } }
        __syncthreads();
    }
    return bi[0];
}

__global__ void k_gu(const float* r, const float* h2, const bf16_t* wg, const bf16_t* wu, float* act) {
    __shared__ float bv[256], xs[D], rg[4][64], ru[4][64]; __shared__ int bi[256];
    int idx = argmax_r(r, bv, bi);
    for (int i = threadIdx.x; i < D; i += 256) xs[i] = h2[(size_t)idx * D + i];
    __syncthreads();
    int l = threadIdx.x & 63, w = threadIdx.x >> 6, j = blockIdx.x * 64 + l;
    float g = 0, u = 0;
    for (int i = w * 160; i < w * 160 + 160; ++i) { g += xs[i] * bf2f(wg[(size_t)i * F + j]); u += xs[i] * bf2f(wu[(size_t)i * F + j]); }
    rg[w][l] = g; ru[w][l] = u;
    __syncthreads();
    if (w == 0) { g = rg[0][l] + rg[1][l] + rg[2][l] + rg[3][l]; u = ru[0][l] + ru[1][l] + ru[2][l] + ru[3][l]; act[j] = g / (1.f + __expf(-g)) * u; }
}

__global__ void k_down(const float* r, const float* h2, const float* x2, const float* mod, const float* act, const bf16_t* wd, bf16_t* out) {
    __shared__ float bv[256], as[F], rg[4][64]; __shared__ int bi[256];
    int idx = argmax_r(r, bv, bi);
    for (int i = threadIdx.x; i < F; i += 256) as[i] = act[i];
    __syncthreads();
    int l = threadIdx.x & 63, w = threadIdx.x >> 6, j = blockIdx.x * 64 + l;
    float g = 0;
    for (int i = w * 384; i < w * 384 + 384; ++i) g += as[i] * bf2f(wd[(size_t)i * D + j]);
    rg[w][l] = g;
    __syncthreads();
    if (w == 0) { g = rg[0][l] + rg[1][l] + rg[2][l] + rg[3][l]; float sg = 1.f / (1.f + __expf(-r[idx]));
        out[(size_t)idx * D + j] = f2bf(x2[(size_t)idx * D + j] + mod[5 * D + j] * (h2[(size_t)idx * D + j] + g * sg)); }
}

extern "C" void kernel_launch(void* const* d_in, const int* in_sizes, int n_in, void* d_out, int out_size, void* d_ws, size_t ws_size, hipStream_t stream) {
    const bf16_t* x = (const bf16_t*)d_in[0]; const bf16_t* awr = (const bf16_t*)d_in[1]; const bf16_t* awi = (const bf16_t*)d_in[2];
    const bf16_t* bwr = (const bf16_t*)d_in[4]; const bf16_t* wg = (const bf16_t*)d_in[5]; const bf16_t* wu = (const bf16_t*)d_in[6];
    const bf16_t* wd = (const bf16_t*)d_in[7]; const bf16_t* c = (const bf16_t*)d_in[8]; const bf16_t* wmod = (const bf16_t*)d_in[9];
    bf16_t* out = (bf16_t*)d_out;
    char* ws = (char*)d_ws;
    float* mod = (float*)ws; float* r = (float*)(ws + 65536); float* act = (float*)(ws + 131072);
    float2* X1 = (float2*)(ws + (1 << 20)); float* h2 = (float*)(ws + (8 << 20)); float* x2 = (float*)(ws + (16 << 20));
    k_mod<<<60, 256, 0, stream>>>(c, wmod, mod);
    k_row1<<<S, 320, 0, stream>>>(x, mod, X1);
    k_col<<<DF, 256, 0, stream>>>(X1, awr, awi);
    k_row2<<<S, 320, 0, stream>>>(x, mod, X1, bwr, out, h2, x2, r);
    k_gu<<<F / 64, 256, 0, stream>>>(r, h2, wg, wu, act);
    k_down<<<D / 64, 256, 0, stream>>>(r, h2, x2, mod, act, wd, out);
}
```

```cpp
#include <hip/hip_runtime.h>
#include <stdint.h>

typedef unsigned short bf16_t;
constexpr int S = 2048, D = 640, DF = 321, F = 1536;

__device__ __forceinline__ float bf2f(bf16_t v) { return __uint_as_float(((unsigned)v) << 16); }
__device__ __forceinline__ bf16_t f2bf(float f) { unsigned u = __float_as_uint(f); return (bf16_t)((u + 0x7fffu + ((u >> 16) & 1u)) >> 16); }
__device__ __forceinline__ float wsum(float v) { for (int o = 32; o; o >>= 1) v += __shfl_xor(v, o); return v; }

__global__ void k_mod(const bf16_t* c, const bf16_t* wmod, float* mod) {
    __shared__ float sc[D];
    for (int i = threadIdx.x; i < D; i += blockDim.x) { float v = bf2f(c[i]); sc[i] = v / (1.f + __expf(-v)); }
    __syncthreads();
    int j = blockIdx.x * 64 + (threadIdx.x & 63), w = threadIdx.x >> 6;
    float a = 0;
    for (int i = w * 160; i < w * 160 + 160; ++i) a += sc[i] * bf2f(wmod[(size_t)i * 6 * D + j]);
    __shared__ float red[4][64];
    red[w][threadIdx.x & 63] = a;
    __syncthreads();
    if (w == 0) mod[j] = red[0][threadIdx.x] + red[1][threadIdx.x] + red[2][threadIdx.x] + red[3][threadIdx.x];
}

__device__ float block_sum(float v, float* red) {
    v = wsum(v);
    __syncthreads();
    if ((threadIdx.x & 63) == 0) red[threadIdx.x >> 6] = v;
    __syncthreads();
    float t = 0;
    for (int i = 0; i < (int)(blockDim.x >> 6); ++i) t += red[i];
    return t;
}

__global__ void k_row1(const bf16_t* x, const float* mod, float2* X1) {
    __shared__ float h[D], ct[D], st[D], red[8];
    int s = blockIdx.x, t = threadIdx.x;
    float v0 = bf2f(x[(size_t)s * D + t]), v1 = bf2f(x[(size_t)s * D + t + 320]);
    float m = block_sum(v0 + v1, red) / D;
    float var = block_sum((v0 - m) * (v0 - m) + (v1 - m) * (v1 - m), red) / D;
    float rs = rsqrtf(var + 1e-5f);
    h[t] = (v0 - m) * rs * (1.f + mod[D + t]) + mod[t];
    h[t + 320] = (v1 - m) * rs * (1.f + mod[D + t + 320]) + mod[t + 320];
    for (int i = t; i < D; i += 320) { float sn, cs; sincospif(2.f * i / D, &sn, &cs); ct[i] = cs; st[i] = sn; }
    __syncthreads();
    for (int k = t; k < DF; k += 320) {
        float re = 0, im = 0; int idx = 0;
        for (int d = 0; d < D; ++d) { re += h[d] * ct[idx]; im -= h[d] * st[idx]; idx += k; if (idx >= D) idx -= D; }
        X1[(size_t)k * S + s] = make_float2(re, im);
    }
}

__device__ void fft2048(float2* a, float2* b, const float2* tw, float sgn) {
    float2 *src = a, *dst = b;
    for (int Ns = 1; Ns < S; Ns <<= 1) {
        for (int j = threadIdx.x; j < S / 2; j += blockDim.x) {
            int k = j & (Ns - 1);
            float2 w = tw[k * (S / (2 * Ns))]; w.y *= sgn;
            float2 u = src[j], v = src[j + S / 2];
            float2 vw = make_float2(v.x * w.x - v.y * w.y, v.x * w.y + v.y * w.x);
            int o = (j - k) * 2 + k;
            dst[o] = make_float2(u.x + vw.x, u.y + vw.y);
            dst[o + Ns] = make_float2(u.x - vw.x, u.y - vw.y);
        }
        __syncthreads();
        float2* t = src; src = dst; dst = t;
    }
}

__global__ void k_col(float2* X1, const bf16_t* wr, const bf16_t* wi) {
    __shared__ float2 a[S], b[S], tw[S / 2];
    int k = blockIdx.x;
    for (int i = threadIdx.x; i < S; i += blockDim.x) a[i] = X1[(size_t)k * S + i];
    for (int i = threadIdx.x; i < S / 2; i += blockDim.x) { float sn, cs; sincospif(2.f * i / S, &sn, &cs); tw[i] = make_float2(cs, sn); }
    __syncthreads();
    fft2048(a, b, tw, -1.f);
    for (int i = threadIdx.x; i < S; i += blockDim.x) {
        float2 v = b[i]; float r = bf2f(wr[(size_t)i * DF + k]), q = bf2f(wi[(size_t)i * DF + k]);
        b[i] = make_float2(v.x * r - v.y * q, v.x * q + v.y * r);
    }
    __syncthreads();
    fft2048(b, a, tw, 1.f);
    for (int i = threadIdx.x; i < S; i += blockDim.x) X1[(size_t)k * S + i] = a[i];
}

__global__ void k_row2(const bf16_t* x, const float* mod, const float2* X1, const bf16_t* wr, bf16_t* out, float* h2, float* x2, float* r) {
    __shared__ float2 z[DF];
    __shared__ float ct[D], st[D], red[8];
    int s = blockIdx.x, t = threadIdx.x;
    for (int k = t; k < DF; k += 320) z[k] = X1[(size_t)k * S + s];
    for (int i = t; i < D; i += 320) { float sn, cs; sincospif(2.f * i / D, &sn, &cs); ct[i] = cs; st[i] = sn; }
    __syncthreads();
    float xv[2];
    for (int q = 0; q < 2; ++q) {
        int d = t + 320 * q;
        float a = 0; int idx = d;
        for (int k = 1; k < 320; ++k) { a += z[k].x * ct[idx] - z[k].y * st[idx]; idx += d; if (idx >= D) idx -= D; }
        a = 2.f * a + z[0].x + ((d & 1) ? -z[320].x : z[320].x);
        a *= 1.f / (S * D);
        xv[q] = bf2f(x[(size_t)s * D + d]) + mod[2 * D + d] * a;
    }
    float m = block_sum(xv[0] + xv[1], red) / D;
    float var = block_sum((xv[0] - m) * (xv[0] - m) + (xv[1] - m) * (xv[1] - m), red) / D;
    float rs = rsqrtf(var + 1e-5f), rp = 0;
    for (int q = 0; q < 2; ++q) {
        int d = t + 320 * q;
        float h = (xv[q] - m) * rs * (1.f + mod[4 * D + d]) + mod[3 * D + d];
        rp += h * bf2f(wr[d]);
        h2[(size_t)s * D + d] = h; x2[(size_t)s * D + d] = xv[q];
        out[(size_t)s * D + d] = f2bf(xv[q] + mod[5 * D + d] * h);
    }
    rp = block_sum(rp, red);
    if (t == 0) r[s] = rp;
}

__device__ int argmax_r(const float* r, float* bv, int* bi) {
    float best = -INFINITY; int idx = 0;
    for (int i = threadIdx.x; i < S; i += blockDim.x) { float v = r[i]; if (v > best) { best = v; idx = i; } }
    bv[threadIdx.x] = best; bi[threadIdx.x] = idx;
    __syncthreads();
    for (int o = blockDim.x / 2; o; o >>= 1) {
        if (threadIdx.x < o) { float v = bv[threadIdx.x + o]; int j = bi[threadIdx.x + o];
            if (v > bv[threadIdx.x] || (v == bv[threadIdx.x] && j < bi[threadIdx.x])) { bv[threadIdx.x] = v; bi[threadIdx.x] = j; } }
        __syncthreads();
    }
    return bi[0];
}

__global__ void k_gu(const float* r, const float* h2, const bf16_t* wg, const bf16_t* wu, float* act) {
    __shared__ float bv[256], xs[D], rg[4][64], ru[4][64]; __shared__ int bi[256];
    int idx = argmax_r(r, bv, bi);
    for (int i = threadIdx.x; i < D; i += 256) xs[i] = h2[(size_t)idx * D + i];
    __syncthreads();
    int l = threadIdx.x & 63, w = threadIdx.x >> 6, j = blockIdx.x * 64 + l;
    float g = 0, u = 0;
    for (int i = w * 160; i < w * 160 + 160; ++i) { g += xs[i] * bf2f(wg[(size_t)i * F + j]); u += xs[i] * bf2f(wu[(size_t)i * F + j]); }
    rg[w][l] = g; ru[w][l] = u;
    __syncthreads();
    if (w == 0) { g = rg[0][l] + rg[1][l] + rg[2][l] + rg[3][l]; u = ru[0][l] + ru[1][l] + ru[2][l] + ru[3][l]; act[j] = g / (1.f + __expf(-g)) * u; }
}

__global__ void k_down(const float* r, const float* h2, const float* x2, const float* mod, const float* act, const bf16_t* wd, bf16_t* out) {
    __shared__ float bv[256], as[F], rg[4][64]; __shared__ int bi[256];
    int idx = argmax_r(r, bv, bi);
    for (int i = threadIdx.x; i < F; i += 256) as[i] = act[i];
    __syncthreads();
    int l = threadIdx.x & 63, w = threadIdx.x >> 6, j = blockIdx.x * 64 + l;
    float g = 0;
    for (int i = w * 384; i < w * 384 + 384; ++i) g += as[i] * bf2f(wd[(size_t)i * D + j]);
    rg[w][l] = g;
    __syncthreads();
    if (w == 0) { g = rg[0][l] + rg[1][l] + rg[2][l] + rg[3][l]; float sg = 1.f / (1.f + __expf(-r[idx]));
        out[(size_t)idx * D + j] = f2bf(x2[(size_t)idx * D + j] + mod[5 * D + j] * (h2[(size_t)idx * D + j] + g * sg)); }
}

#define MODE 1
#define METRIC (mx0 - mn)

typedef unsigned long long u64;
typedef unsigned v4u __attribute__((ext_vector_type(4)));
__device__ __forceinline__ u64 rt() { u64 t; asm volatile("s_memrealtime %0\n\ts_waitcnt lgkmcnt(0)" : "=s"(t) :: "memory"); return t; }
#define R4(x) x x x x
#define R16(x) R4(R4(x))
#define R64(x) R4(R16(x))
#define R256(x) R4(R64(x))
#define BLK(a, b, c, d, e) asm volatile(R256("v_fma_f32 %0, %0, %4, %4\n\tv_fma_f32 %1, %1, %4, %4\n\tv_fma_f32 %2, %2, %4, %4\n\tv_fma_f32 %3, %3, %4, %4\n\t") : "+v"(a), "+v"(b), "+v"(c), "+v"(d) : "v"(e))
template <int NT> __device__ __forceinline__ v4u ldv(const v4u* p, int i, int n) { v4u z = {0, 0, 0, 0}; if (i < n) { if (NT) z = __builtin_nontemporal_load(p + i); else z = p[i]; } return z; }
__global__ void __launch_bounds__(1024) k_probe(const v4u* x, const v4u* awr, const v4u* awi, const v4u* wg, const v4u* wu, const v4u* wd, const v4u* wmod, u64* ts) {
    const u64 r0 = rt();
    const int i = blockIdx.x * 1024 + threadIdx.x;
    const int j = blockIdx.x * 640 + threadIdx.x;
    v4u acc = {0, 0, 0, 0};
    asm volatile("" :: "v"(acc));
    const u64 r1 = rt();
    if ((threadIdx.x & 63) == 0) { u64* o = ts + ((size_t)blockIdx.x * 16 + (threadIdx.x >> 6)) * 2; o[0] = r0; o[1] = r1; }
}
__global__ void __launch_bounds__(64) k_amp(const u64* ts) {
    u64 mn = ~0ull, mx0 = 0, mx1 = 0, md = 0;
    for (int i = threadIdx.x; i < 4096; i += 64) { u64 a = ts[2 * i], b = ts[2 * i + 1]; mn = a < mn ? a : mn; mx0 = a > mx0 ? a : mx0; mx1 = b > mx1 ? b : mx1; md = b - a > md ? b - a : md; }
    for (int o = 32; o; o >>= 1) { u64 t = __shfl_xor(mn, o); mn = t < mn ? t : mn; t = __shfl_xor(mx0, o); mx0 = t > mx0 ? t : mx0; t = __shfl_xor(mx1, o); mx1 = t > mx1 ? t : mx1; t = __shfl_xor(md, o); md = t > md ? t : md; }
    const u64 now = rt();
    u64 metric = METRIC;
    if (metric > 3000) metric = 3000;
    while (rt() < now + metric * 500) __builtin_amdgcn_s_sleep(8);
}

extern "C" void kernel_launch(void* const* d_in, const int* in_sizes, int n_in, void* d_out, int out_size, void* d_ws, size_t ws_size, hipStream_t stream) {
    const bf16_t* x = (const bf16_t*)d_in[0]; const bf16_t* awr = (const bf16_t*)d_in[1]; const bf16_t* awi = (const bf16_t*)d_in[2];
    const bf16_t* bwr = (const bf16_t*)d_in[4]; const bf16_t* wg = (const bf16_t*)d_in[5]; const bf16_t* wu = (const bf16_t*)d_in[6];
    const bf16_t* wd = (const bf16_t*)d_in[7]; const bf16_t* c = (const bf16_t*)d_in[8]; const bf16_t* wmod = (const bf16_t*)d_in[9];
    bf16_t* out = (bf16_t*)d_out;
    char* ws = (char*)d_ws;
    float* mod = (float*)ws; float* r = (float*)(ws + 65536); float* act = (float*)(ws + 131072);
    float2* X1 = (float2*)(ws + (1 << 20)); float* h2 = (float*)(ws + (8 << 20)); float* x2 = (float*)(ws + (16 << 20));
    u64* ts = (u64*)(ws + (32 << 20));
    k_probe<<<256, 1024, 0, stream>>>((const v4u*)x, (const v4u*)awr, (const v4u*)awi, (const v4u*)wg, (const v4u*)wu, (const v4u*)wd, (const v4u*)wmod, ts);
    k_amp<<<1, 64, 0, stream>>>(ts);
    k_mod<<<60, 256, 0, stream>>>(c, wmod, mod);
    k_row1<<<S, 320, 0, stream>>>(x, mod, X1);
    k_col<<<DF, 256, 0, stream>>>(X1, awr, awi);
    k_row2<<<S, 320, 0, stream>>>(x, mod, X1, bwr, out, h2, x2, r);
    k_gu<<<F / 64, 256, 0, stream>>>(r, h2, wg, wu, act);
    k_down<<<D / 64, 256, 0, stream>>>(r, h2, x2, mod, act, wd, out);
}
```

```cpp
#include <hip/hip_runtime.h>
#include <stdint.h>

typedef unsigned short bf16_t;
constexpr int S = 2048, D = 640, DF = 321, F = 1536;

__device__ __forceinline__ float bf2f(bf16_t v) { return __uint_as_float(((unsigned)v) << 16); }
__device__ __forceinline__ bf16_t f2bf(float f) { unsigned u = __float_as_uint(f); return (bf16_t)((u + 0x7fffu + ((u >> 16) & 1u)) >> 16); }
__device__ __forceinline__ float wsum(float v) { for (int o = 32; o; o >>= 1) v += __shfl_xor(v, o); return v; }

__global__ void k_mod(const bf16_t* c, const bf16_t* wmod, float* mod) {
    __shared__ float sc[D];
    for (int i = threadIdx.x; i < D; i += blockDim.x) { float v = bf2f(c[i]); sc[i] = v / (1.f + __expf(-v)); }
    __syncthreads();
    int j = blockIdx.x * 64 + (threadIdx.x & 63), w = threadIdx.x >> 6;
    float a = 0;
    for (int i = w * 160; i < w * 160 + 160; ++i) a += sc[i] * bf2f(wmod[(size_t)i * 6 * D + j]);
    __shared__ float red[4][64];
    red[w][threadIdx.x & 63] = a;
    __syncthreads();
    if (w == 0) mod[j] = red[0][threadIdx.x] + red[1][threadIdx.x] + red[2][threadIdx.x] + red[3][threadIdx.x];
}

__device__ float block_sum(float v, float* red) {
    v = wsum(v);
    __syncthreads();
    if ((threadIdx.x & 63) == 0) red[threadIdx.x >> 6] = v;
    __syncthreads();
    float t = 0;
    for (int i = 0; i < (int)(blockDim.x >> 6); ++i) t += red[i];
    return t;
}

__global__ void k_row1(const bf16_t* x, const float* mod, float2* X1) {
    __shared__ float h[D], ct[D], st[D], red[8];
    int s = blockIdx.x, t = threadIdx.x;
    float v0 = bf2f(x[(size_t)s * D + t]), v1 = bf2f(x[(size_t)s * D + t + 320]);
    float m = block_sum(v0 + v1, red) / D;
    float var = block_sum((v0 - m) * (v0 - m) + (v1 - m) * (v1 - m), red) / D;
    float rs = rsqrtf(var + 1e-5f);
    h[t] = (v0 - m) * rs * (1.f + mod[D + t]) + mod[t];
    h[t + 320] = (v1 - m) * rs * (1.f + mod[D + t + 320]) + mod[t + 320];
    for (int i = t; i < D; i += 320) { float sn, cs; sincospif(2.f * i / D, &sn, &cs); ct[i] = cs; st[i] = sn; }
    __syncthreads();
    for (int k = t; k < DF; k += 320) {
        float re = 0, im = 0; int idx = 0;
        for (int d = 0; d < D; ++d) { re += h[d] * ct[idx]; im -= h[d] * st[idx]; idx += k; if (idx >= D) idx -= D; }
        X1[(size_t)k * S + s] = make_float2(re, im);
    }
}

__device__ void fft2048(float2* a, float2* b, const float2* tw, float sgn) {
    float2 *src = a, *dst = b;
    for (int Ns = 1; Ns < S; Ns <<= 1) {
        for (int j = threadIdx.x; j < S / 2; j += blockDim.x) {
            int k = j & (Ns - 1);
            float2 w = tw[k * (S / (2 * Ns))]; w.y *= sgn;
            float2 u = src[j], v = src[j + S / 2];
            float2 vw = make_float2(v.x * w.x - v.y * w.y, v.x * w.y + v.y * w.x);
            int o = (j - k) * 2 + k;
            dst[o] = make_float2(u.x + vw.x, u.y + vw.y);
            dst[o + Ns] = make_float2(u.x - vw.x, u.y - vw.y);
        }
        __syncthreads();
        float2* t = src; src = dst; dst = t;
    }
}

__global__ void k_col(float2* X1, const bf16_t* wr, const bf16_t* wi) {
    __shared__ float2 a[S], b[S], tw[S / 2];
    int k = blockIdx.x;
    for (int i = threadIdx.x; i < S; i += blockDim.x) a[i] = X1[(size_t)k * S + i];
    for (int i = threadIdx.x; i < S / 2; i += blockDim.x) { float sn, cs; sincospif(2.f * i / S, &sn, &cs); tw[i] = make_float2(cs, sn); }
    __syncthreads();
    fft2048(a, b, tw, -1.f);
    for (int i = threadIdx.x; i < S; i += blockDim.x) {
        float2 v = b[i]; float r = bf2f(wr[(size_t)i * DF + k]), q = bf2f(wi[(size_t)i * DF + k]);
        b[i] = make_float2(v.x * r - v.y * q, v.x * q + v.y * r);
    }
    __syncthreads();
    fft2048(b, a, tw, 1.f);
    for (int i = threadIdx.x; i < S; i += blockDim.x) X1[(size_t)k * S + i] = a[i];
}

__global__ void k_row2(const bf16_t* x, const float* mod, const float2* X1, const bf16_t* wr, bf16_t* out, float* h2, float* x2, float* r) {
    __shared__ float2 z[DF];
    __shared__ float ct[D], st[D], red[8];
    int s = blockIdx.x, t = threadIdx.x;
    for (int k = t; k < DF; k += 320) z[k] = X1[(size_t)k * S + s];
    for (int i = t; i < D; i += 320) { float sn, cs; sincospif(2.f * i / D, &sn, &cs); ct[i] = cs; st[i] = sn; }
    __syncthreads();
    float xv[2];
    for (int q = 0; q < 2; ++q) {
        int d = t + 320 * q;
        float a = 0; int idx = d;
        for (int k = 1; k < 320; ++k) { a += z[k].x * ct[idx] - z[k].y * st[idx]; idx += d; if (idx >= D) idx -= D; }
        a = 2.f * a + z[0].x + ((d & 1) ? -z[320].x : z[320].x);
        a *= 1.f / (S * D);
        xv[q] = bf2f(x[(size_t)s * D + d]) + mod[2 * D + d] * a;
    }
    float m = block_sum(xv[0] + xv[1], red) / D;
    float var = block_sum((xv[0] - m) * (xv[0] - m) + (xv[1] - m) * (xv[1] - m), red) / D;
    float rs = rsqrtf(var + 1e-5f), rp = 0;
    for (int q = 0; q < 2; ++q) {
        int d = t + 320 * q;
        float h = (xv[q] - m) * rs * (1.f + mod[4 * D + d]) + mod[3 * D + d];
        rp += h * bf2f(wr[d]);
        h2[(size_t)s * D + d] = h; x2[(size_t)s * D + d] = xv[q];
        out[(size_t)s * D + d] = f2bf(xv[q] + mod[5 * D + d] * h);
    }
    rp = block_sum(rp, red);
    if (t == 0) r[s] = rp;
}

__device__ int argmax_r(const float* r, float* bv, int* bi) {
    float best = -INFINITY; int idx = 0;
    for (int i = threadIdx.x; i < S; i += blockDim.x) { float v = r[i]; if (v > best) { best = v; idx = i; } }
    bv[threadIdx.x] = best; bi[threadIdx.x] = idx;
    __syncthreads();
    for (int o = blockDim.x / 2; o; o >>= 1) {
        if (threadIdx.x < o) { float v = bv[threadIdx.x + o]; int j = bi[threadIdx.x + o];
            if (v > bv[threadIdx.x] || (v == bv[threadIdx.x] && j < bi[threadIdx.x])) { bv[threadIdx.x] = v; bi[threadIdx.x] = j; } }
        __syncthreads();
    }
    return bi[0];
}

__global__ void k_gu(const float* r, const float* h2, const bf16_t* wg, const bf16_t* wu, float* act) {
    __shared__ float bv[256], xs[D], rg[4][64], ru[4][64]; __shared__ int bi[256];
    int idx = argmax_r(r, bv, bi);
    for (int i = threadIdx.x; i < D; i += 256) xs[i] = h2[(size_t)idx * D + i];
    __syncthreads();
    int l = threadIdx.x & 63, w = threadIdx.x >> 6, j = blockIdx.x * 64 + l;
    float g = 0, u = 0;
    for (int i = w * 160; i < w * 160 + 160; ++i) { g += xs[i] * bf2f(wg[(size_t)i * F + j]); u += xs[i] * bf2f(wu[(size_t)i * F + j]); }
    rg[w][l] = g; ru[w][l] = u;
    __syncthreads();
    if (w == 0) { g = rg[0][l] + rg[1][l] + rg[2][l] + rg[3][l]; u = ru[0][l] + ru[1][l] + ru[2][l] + ru[3][l]; act[j] = g / (1.f + __expf(-g)) * u; }
}

__global__ void k_down(const float* r, const float* h2, const float* x2, const float* mod, const float* act, const bf16_t* wd, bf16_t* out) {
    __shared__ float bv[256], as[F], rg[4][64]; __shared__ int bi[256];
    int idx = argmax_r(r, bv, bi);
    for (int i = threadIdx.x; i < F; i += 256) as[i] = act[i];
    __syncthreads();
    int l = threadIdx.x & 63, w = threadIdx.x >> 6, j = blockIdx.x * 64 + l;
    float g = 0;
    for (int i = w * 384; i < w * 384 + 384; ++i) g += as[i] * bf2f(wd[(size_t)i * D + j]);
    rg[w][l] = g;
    __syncthreads();
    if (w == 0) { g = rg[0][l] + rg[1][l] + rg[2][l] + rg[3][l]; float sg = 1.f / (1.f + __expf(-r[idx]));
        out[(size_t)idx * D + j] = f2bf(x2[(size_t)idx * D + j] + mod[5 * D + j] * (h2[(size_t)idx * D + j] + g * sg)); }
}

#define NTHR 512
#define METRIC (0)

typedef unsigned long long u64;
__device__ __forceinline__ u64 rt() { u64 t; asm volatile("s_memrealtime %0\n\ts_waitcnt lgkmcnt(0)" : "=s"(t) :: "memory"); return t; }
__global__ void __launch_bounds__(NTHR) k_probe(u64* ts) {
    extern __shared__ unsigned dl[];
    const u64 r0 = rt();
    if ((threadIdx.x & 63) == 0) { u64* o = ts + ((size_t)blockIdx.x * 16 + (threadIdx.x >> 6)) * 2; o[0] = r0; o[1] = r0; }
}
__global__ void __launch_bounds__(64) k_amp(const u64* ts, const u64* ts2, const u64* prev) {
    u64 mn = ~0ull, mx0 = 0, mn2 = ~0ull, mx2 = 0, pe = 0;
    for (int i = threadIdx.x; i < 4096; i += 64) if ((i & 15) < NTHR / 64) { u64 a = ts[2 * i], b = ts2[2 * i], c = prev[2 * i]; mn = a < mn ? a : mn; mx0 = a > mx0 ? a : mx0; mn2 = b < mn2 ? b : mn2; mx2 = b > mx2 ? b : mx2; pe = c > pe ? c : pe; }
    for (int o = 32; o; o >>= 1) { u64 t = __shfl_xor(mn, o); mn = t < mn ? t : mn; t = __shfl_xor(mx0, o); mx0 = t > mx0 ? t : mx0; t = __shfl_xor(mn2, o); mn2 = t < mn2 ? t : mn2; t = __shfl_xor(mx2, o); mx2 = t > mx2 ? t : mx2; t = __shfl_xor(pe, o); pe = t > pe ? t : pe; }
    const u64 now = rt();
    u64 metric = METRIC;
    if (metric > 2000) metric = 0;
    while (rt() < now + metric * 500) __builtin_amdgcn_s_sleep(8);
}

extern "C" void kernel_launch(void* const* d_in, const int* in_sizes, int n_in, void* d_out, int out_size, void* d_ws, size_t ws_size, hipStream_t stream) {
    const bf16_t* x = (const bf16_t*)d_in[0]; const bf16_t* awr = (const bf16_t*)d_in[1]; const bf16_t* awi = (const bf16_t*)d_in[2];
    const bf16_t* bwr = (const bf16_t*)d_in[4]; const bf16_t* wg = (const bf16_t*)d_in[5]; const bf16_t* wu = (const bf16_t*)d_in[6];
    const bf16_t* wd = (const bf16_t*)d_in[7]; const bf16_t* c = (const bf16_t*)d_in[8]; const bf16_t* wmod = (const bf16_t*)d_in[9];
    bf16_t* out = (bf16_t*)d_out;
    char* ws = (char*)d_ws;
    float* mod = (float*)ws; float* r = (float*)(ws + 65536); float* act = (float*)(ws + 131072);
    float2* X1 = (float2*)(ws + (1 << 20)); float* h2 = (float*)(ws + (8 << 20)); float* x2 = (float*)(ws + (16 << 20));
    u64* ts = (u64*)(ws + (32 << 20)); u64* ts2 = (u64*)(ws + (33 << 20)); u64* ts3 = (u64*)(ws + (34 << 20));
    hipFuncSetAttribute((const void*)k_probe, hipFuncAttributeMaxDynamicSharedMemorySize, 98304);
    k_probe<<<256, NTHR, 98304, stream>>>(ts);
    k_probe<<<256, NTHR, 98304, stream>>>(ts2);
    k_amp<<<1, 64, 0, stream>>>(ts, ts2, ts3);
    k_mod<<<60, 256, 0, stream>>>(c, wmod, mod);
    k_row1<<<S, 320, 0, stream>>>(x, mod, X1);
    k_col<<<DF, 256, 0, stream>>>(X1, awr, awi);
    k_row2<<<S, 320, 0, stream>>>(x, mod, X1, bwr, out, h2, x2, r);
    k_gu<<<F / 64, 256, 0, stream>>>(r, h2, wg, wu, act);
    k_down<<<D / 64, 256, 0, stream>>>(r, h2, x2, mod, act, wd, out);
    k_probe<<<256, NTHR, 98304, stream>>>(ts3);
}
```

```cpp
#include <hip/hip_runtime.h>
#include <stdint.h>
#pragma clang diagnostic ignored "-Wint-to-pointer-cast"

typedef _Float16 h2 __attribute__((ext_vector_type(2)));
typedef _Float16 h8 __attribute__((ext_vector_type(8)));
typedef __bf16 b2 __attribute__((ext_vector_type(2)));
typedef __bf16 b8 __attribute__((ext_vector_type(8)));
typedef short s4 __attribute__((ext_vector_type(4)));
typedef float f2 __attribute__((ext_vector_type(2)));
typedef float f4 __attribute__((ext_vector_type(4)));
typedef unsigned u2 __attribute__((ext_vector_type(2)));
typedef unsigned u4 __attribute__((ext_vector_type(4)));
typedef unsigned long long ul2 __attribute__((ext_vector_type(2)));
typedef unsigned long long ul4 __attribute__((ext_vector_type(4)));
typedef __amdgpu_buffer_rsrc_t rsrc_t;
#define LAS __attribute__((address_space(3)))
#define DEV __device__ __forceinline__

#define STAMP 0
#define D_CA 0
#define D_W 2
#define D_X1 0
#define D_X2 0
#define D_KEY 2
#define D_KEYB 10
#define D_YP 8
#define D_CAN 0
#define PF_WG 4

constexpr int WS_EPOCH = 0;
constexpr int WS_BASE = 1024;
constexpr int WS_KEY = 4096;
constexpr int WS_CA = WS_KEY + 32768;
constexpr int WS_SA = WS_CA + 81920;
constexpr int WS_MODR = WS_SA + 81920;
constexpr int WS_YP = 262144;
constexpr int WS_ROW = WS_YP + 245760;
constexpr int XSTR = 16640;
constexpr int WS_X = 1 << 20;
constexpr int WS_END = WS_X + 161 * XSTR;
constexpr int WS_DBG = 8 << 20;
static_assert(WS_MODR + 40960 <= WS_YP && WS_ROW + 524288 <= WS_X && WS_END < WS_DBG, "workspace map");

constexpr int L_N = 0, NP = 1312;
constexpr int L_CA = 10496, CAP = 144;
constexpr int L_S = L_CA + 2 * 2304;
constexpr int L_SU = L_S + 1536;
constexpr int L_MISC = L_SU + 512;
constexpr int L_H = L_MISC + 1024;
constexpr int L_MOD = L_H + 1536;
constexpr int L_E3 = L_MOD + 6144, E3P = 144;
constexpr int L_UT = L_E3 + 128 * E3P;
constexpr int L_SINK = L_UT + 10240;
constexpr int L_E1 = L_SINK + 256, E1P = 1040;
constexpr int L_E2 = L_E1 + 16640, E2P = 4128;
constexpr int L_MT = L_E1;
constexpr int L_GU = L_MT + 40960;
constexpr int L_END = L_GU + 40960;
static_assert(L_E2 + 4 * E2P <= L_GU && L_E1 % 16 == 0 && L_E3 % 16 == 0 && L_END + 2048 <= 163840, "LDS map");
constexpr int M_STAT = L_MISC, M_WIN = L_MISC + 128, M_PART = L_MISC + 256;

constexpr int OOB = 0x7ffffff0;
constexpr unsigned SENT = 0xffffffffu, LSB2 = 0x00010001u;
constexpr int SPIN = 1 << 15;
constexpr int SC1 = 16;
constexpr int W_DUAL = 88, W_SA = 89;

DEV h2 as_h2(unsigned v) { return __builtin_bit_cast(h2, v); }
DEV b2 as_b2(unsigned v) { return __builtin_bit_cast(b2, v); }
DEV unsigned as_u(h2 v) { return __builtin_bit_cast(unsigned, v); }
DEV unsigned as_u(float v) { return __builtin_bit_cast(unsigned, v); }
DEV float as_f(unsigned v) { return __builtin_bit_cast(float, v); }
DEV f4 as_f4(u4 v) { return __builtin_bit_cast(f4, v); }
DEV unsigned pkh(float a, float b) { f2 p = {a, b}; return as_u(__builtin_convertvector(p, h2)); }
DEV unsigned pkb(float a, float b) { f2 p = {a, b}; return __builtin_bit_cast(unsigned, __builtin_convertvector(p, b2)); }
DEV float bflo(unsigned v) { return as_f(v << 16); }
DEV float bfhi(unsigned v) { return as_f(v & 0xffff0000u); }
DEV f4 unb(u2 v) { const unsigned a = v[0], b = v[1]; f4 r = {bflo(a), bfhi(a), bflo(b), bfhi(b)}; return r; }
DEV u2 pkb4(f4 v) { return u2{pkb(v[0], v[1]), pkb(v[2], v[3])}; }
DEV u2 pkh4(f4 v) { return u2{pkh(v[0], v[1]), pkh(v[2], v[3])}; }
DEV f4 mfma(u4 a, u4 b, f4 c) { return __builtin_amdgcn_mfma_f32_16x16x32_f16(__builtin_bit_cast(h8, a), __builtin_bit_cast(h8, b), c, 0, 0, 0); }
DEV f4 mfmab(u4 a, u4 b, f4 c) { return __builtin_amdgcn_mfma_f32_16x16x32_bf16(__builtin_bit_cast(b8, a), __builtin_bit_cast(b8, b), c, 0, 0, 0); }
DEV u4 pack4(f4 re, f4 im) { u4 r = {pkh(re[0], im[0]), pkh(re[1], im[1]), pkh(re[2], im[2]), pkh(re[3], im[3])}; return r; }
DEV u4 cmul_pack(f4 re, f4 im, f4 wr, f4 wi) { return pack4(re * wr - im * wi, re * wi + im * wr); }
DEV rsrc_t mkrs(const void* p, int n) { return __builtin_amdgcn_make_buffer_rsrc((void*)p, 0, n, 0x00020000); }
template <int AUX> DEV u4 ld4(rsrc_t r, int o, int so = 0) { return __builtin_bit_cast(u4, __builtin_amdgcn_raw_buffer_load_b128(r, o, so, AUX)); }
template <int AUX> DEV u2 ld2(rsrc_t r, int o, int so = 0) { return __builtin_bit_cast(u2, __builtin_amdgcn_raw_buffer_load_b64(r, o, so, AUX)); }
template <int AUX> DEV unsigned ld1(rsrc_t r, int o) { return __builtin_amdgcn_raw_buffer_load_b32(r, o, 0, AUX); }
template <int AUX> DEV void st4(u4 v, rsrc_t r, int o) { __builtin_amdgcn_raw_buffer_store_b128(v, r, o, 0, AUX); }
template <int AUX> DEV void st2(u2 v, rsrc_t r, int o, int so = 0) { __builtin_amdgcn_raw_buffer_store_b64(v, r, o, so, AUX); }
template <int AUX> DEV void st1(unsigned v, rsrc_t r, int o) { __builtin_amdgcn_raw_buffer_store_b32(v, r, o, 0, AUX); }
template <class T> DEV T lds_ld(int a) { return *(const LAS T*)a; }
template <class T> DEV void lds_st(int a, T v) { *(LAS T*)a = v; }
DEV u2 lds_tr(int a) { return __builtin_bit_cast(u2, __builtin_amdgcn_ds_read_tr16_b64_v4i16((LAS s4*)a)); }
DEV u4 lds_tr2(int a0, int a1) { const u2 x = lds_tr(a0), y = lds_tr(a1); u4 r = {x[0], x[1], y[0], y[1]}; return r; }
DEV void dma16(rsrc_t r, int voff, int soff, int ldsbase) { asm volatile("s_mov_b32 m0, %3\n\ts_nop 0\n\tbuffer_load_dwordx4 %0, %1, %2 offen lds" :: "v"(voff), "s"(r), "s"(soff), "s"(ldsbase) : "memory"); }
DEV void dma4(rsrc_t r, int voff, int ldsbase) { asm volatile("s_mov_b32 m0, %2\n\ts_nop 0\n\tbuffer_load_dword %0, %1, 0 offen lds" :: "v"(voff), "s"(r), "s"(ldsbase) : "memory"); }
#define DRAIN() asm volatile("s_waitcnt vmcnt(0)" ::: "memory")
#define BAR() asm volatile("s_waitcnt lgkmcnt(0)\n\ts_barrier" ::: "memory")
#define FENCE() asm volatile("" ::: "memory")
#define SB() __builtin_amdgcn_sched_barrier(0)
#define PIN(x) asm volatile("" : "+v"(x))
#define PRIO(n) __builtin_amdgcn_s_setprio(n)
template <int N> DEV void nap() { if (N > 0) __builtin_amdgcn_s_sleep(N); }
DEV bool any(bool b) { return __builtin_amdgcn_ballot_w64(b) != 0; }
template <int CTRL> DEV unsigned dpp(unsigned v) { return (unsigned)__builtin_amdgcn_update_dpp(0, (int)v, CTRL, 0xf, 0xf, true); }
template <int CTRL> DEV float dppf(float v) { return as_f(dpp<CTRL>(as_u(v))); }
DEV unsigned rdl(unsigned v, int lane) { return (unsigned)__builtin_amdgcn_readlane((int)v, lane); }
DEV float row_sum(float v) { v += dppf<0x128>(v); v += dppf<0x124>(v); v += dppf<0x122>(v); v += dppf<0x121>(v); return v; }
DEV float fold4(float a, float b, float c, float d) {
    const auto p = __builtin_amdgcn_permlane32_swap(as_u(a), as_u(c), false, false);
    const auto q = __builtin_amdgcn_permlane32_swap(as_u(b), as_u(d), false, false);
    const auto s = __builtin_amdgcn_permlane16_swap(as_u(as_f(p[0]) + as_f(p[1])), as_u(as_f(q[0]) + as_f(q[1])), false, false);
    return as_f(s[0]) + as_f(s[1]);
}
DEV float wave_sum(float v) {
    v = row_sum(v);
    return (as_f(rdl(as_u(v), 0)) + as_f(rdl(as_u(v), 16))) + (as_f(rdl(as_u(v), 32)) + as_f(rdl(as_u(v), 48)));
}
DEV unsigned wave_umax(unsigned v) {
    v = max(v, dpp<0x128>(v)); v = max(v, dpp<0x124>(v)); v = max(v, dpp<0x122>(v)); v = max(v, dpp<0x121>(v));
    return max(max(rdl(v, 0), rdl(v, 16)), max(rdl(v, 32), rdl(v, 48)));
}
DEV f2 lo2(f4 v) { return f2{v[0], v[1]}; }
DEV f2 hi2(f4 v) { return f2{v[2], v[3]}; }
DEV float hsum(f4 v) { const f2 t = lo2(v) + hi2(v); return t[0] + t[1]; }
DEV float silu(float v) { return v * __builtin_amdgcn_rcpf(1.f + __expf(-v)); }
DEV float cosr(float rev) { return __builtin_amdgcn_cosf(rev); }
DEV float sinr(float rev) { return __builtin_amdgcn_sinf(rev); }
DEV f2 cis(float rev) { return f2{cosr(rev), sinr(rev)}; }
template <int N> DEV void cis_run(f2 (&v)[N], float th0, float step, float scale) {
    const float t = 2.f * cosr(step);
    v[0] = cis(th0) * scale; v[1] = cis(th0 + step) * scale;
#pragma unroll
    for (int n = 2; n < N; ++n) v[n] = t * v[n - 1] - v[n - 2];
}
DEV int col_kappa(int q, int cc) { const int pi = cc >> 1, m = pi == 0 ? 20 : 40 - pi; return (q != 0 && (cc & 1)) ? m : pi; }
DEV int col_k1(int q, int cc) { return q == 0 ? 4 * (cc & 1) : q; }
DEV int piece_off(int q, int cc, int g, int p) {
    const int u = cc >> 2, e = cc & 3;
    const int a = ((2 * q + (g >> 1)) * 20 + (g & 1) * 10 + u) * XSTR + e * 16, b = 160 * XSTR + (2 * g + e) * 16;
    const bool extra = q == 0 && u == 10 && e < 2 && g < 2;
    return (u < 10 || extra) ? WS_X + p * 64 + (u < 10 ? a : b) : OOB;
}
DEV void gemv_tile(int img, int sv, int l, f4& a0, f4& a1) {
    const int g = l >> 4, ta = img + (8 * g + ((l >> 2) & 3)) * 64 + (l & 3) * 8;
    u4 sf[5], b0[5], b1[5];
#pragma unroll
    for (int s = 0; s < 5; ++s) {
        sf[s] = lds_ld<u4>(sv + s * 64 + g * 16);
        b0[s] = lds_tr2(ta + s * 2048, ta + s * 2048 + 256); b1[s] = lds_tr2(ta + s * 2048 + 32, ta + s * 2048 + 288);
    }
    SB();
    a0 = f4{0, 0, 0, 0}; a1 = a0;
#pragma unroll
    for (int s = 0; s < 5; ++s) { a0 = mfmab(sf[s], b0[s], a0); a1 = mfmab(sf[s], b1[s], a1); }
}

#define TS(i)
#define TSINIT()
#define TSDUMP()

__global__ void __launch_bounds__(512, 2)
gfnet_mod_kernel(const char* a_x, const char* a_wmod, const char* a_c, char* a_ws, const char* a_wr, const char* a_wi, const char* a_br, char* a_out,
                 const char* a_gate, const char* a_up, const char* a_down) {
    __shared__ __attribute__((aligned(16))) char lds_image[L_END + STAMP * 2048];
    asm volatile("" :: "s"((LAS char*)lds_image));
    ul4 ka, kb; ul2 kc; unsigned long long kd_;
    asm volatile("s_load_dwordx8 %0, %4, 0x0\n\ts_load_dwordx8 %1, %4, 0x20\n\ts_load_dwordx4 %2, %4, 0x40\n\ts_load_dwordx2 %3, %4, 0x50"
                 : "=&s"(ka), "=&s"(kb), "=&s"(kc), "=&s"(kd_) : "s"(__builtin_amdgcn_kernarg_segment_ptr()));
    const int w = blockIdx.x, x8 = w & 7, r = w >> 3, s2 = 32 * x8 + r;
    const int tid = threadIdx.x, wave = __builtin_amdgcn_readfirstlane(tid >> 6), l = tid & 63, g = l >> 4, c = l & 15;
    {
        unsigned long long pc; int len;
        asm volatile("s_getpc_b64 %0\n.Lgf_pc:\n\ts_mov_b32 %1, .Lgf_end-.Lgf_pc" : "=s"(pc), "=s"(len));
        dma4(mkrs((const void*)pc, r < PF_WG ? len : 0), tid * 128, L_SINK);
    }
    const bool lo = wave < 4;
    const int hidx = tid - 320;
    const bool roleB = r >= 12 || w == W_DUAL, roleF = r < 12, roleM = r < 10;
    const f4 zero4 = {0, 0, 0, 0};
    const int uid = 10 * x8 + (r < 10 ? r : r - 12), ukq = (uid >> 1) / 10, ucg = 2 * ((uid >> 1) % 10) + (uid & 1);
    const bool urgent = (wave == 4) & ((r < 10) | ((unsigned)(r - 12) < 10u));
    int ooff = 32 * c + 8 * g;
    int o2 = c < 8 ? 1024 + ooff : OOB;
    int rowb = (256 * wave + s2) * 1280;

    int uo = (ukq * 160 + (l >> 2)) * 7680 + (r < 10 ? 1280 : 0) + ucg * 64 + (l & 3) * 16;
    int cqo = tid * 8 + (wave == 4 ? ukq * 320 - 2048 : -2560);
    PIN(uo); PIN(cqo); PIN(ooff); PIN(o2);
    asm volatile("s_waitcnt lgkmcnt(0)" : "+s"(ka), "+s"(kb), "+s"(kc), "+s"(kd_), "+s"(rowb));
    const char *xin = (const char*)ka[0], *wmod = (const char*)ka[1], *cvec = (const char*)ka[2], *awr = (const char*)kb[0], *awi = (const char*)kb[1], *bwr = (const char*)kb[2];
    const char *wgate = (const char*)kc[0], *wup = (const char*)kc[1], *wdown = (const char*)kd_;
    char *ws = (char*)ka[3], *out = (char*)kb[3];
    const rsrc_t rw = mkrs(ws, WS_END), rx = mkrs(xin, 2048 * 1280), rm = mkrs(wmod, 640 * 7680), rc = mkrs(cvec, 1280);
    u2 cq;
    asm volatile("buffer_load_dwordx2 %0, %1, %2, 0 offen" : "=v"(cq) : "v"(cqo), "s"(rc));
    if (urgent) {
        PRIO(3);
        const int o = uo;
#pragma unroll
        for (int i = 0; i < 10; ++i) dma16(rm, o, i * 16 * 7680, L_UT + i * 1024);
    }
    const unsigned epoch = ld1<SC1>(rw, WS_EPOCH);
    const u2 xr0 = ld2<0>(rx, ooff, rowb), xr1 = ld2<0>(rx, 512 + ooff, rowb), xr2 = ld2<0>(rx, o2, rowb);
    TS(23);

    unsigned tag, xp0;
    f4 xc[3]; float m1;
    auto ln1 = [&]() {
        xc[0] = unb(xr0); xc[1] = unb(xr1); xc[2] = unb(xr2);
        m1 = wave_sum(hsum(xc[0] + xc[1] + xc[2])) * (1.f / 640.f);
        xc[0] -= m1; xc[1] -= m1; xc[2] -= (c < 8 ? m1 : 0.f);
        const float rs = __builtin_amdgcn_rsqf(wave_sum(hsum(xc[0] * xc[0] + xc[1] * xc[1] + xc[2] * xc[2])) * (1.f / 640.f) + 1e-5f);
        const int a = L_N + wave * NP + ooff;
        lds_st<u2>(a, pkh4(xc[0] * rs)); lds_st<u2>(a + 512, pkh4(xc[1] * rs));
        if (c < 8) lds_st<u2>(a + 1024, pkh4(xc[2] * rs));
        TS(2);
        BAR();
        TS(3);
    };
    auto settag = [&]() {
        tag = (unsigned)__builtin_amdgcn_readfirstlane((int)epoch) + 1u;
        xp0 = (tag & 1u) | ((tag & 2u) << 15);
    };
    auto xpat = [&](int e, int k) -> unsigned {
        const unsigned hb = ((tag * 0x9e3779b1u) >> (16 + 4 * e + 2 * (k & 1))), hv = (hb & 1u) | ((hb & 2u) << 15);
        return k == 0 ? xp0 : (k == 1 ? xp0 ^ LSB2 : hv);
    };

    const int bq = w == W_DUAL ? 0 : x8 >> 1, bu = w == W_DUAL ? 10 : (r - 12) % 10, bg = ((x8 & 1) << 1) | (r >= 22);
    const int seg = w == W_DUAL ? 160 : 20 * x8 + r - 12;
    u4 guf[5]; u2 wdr[16];

    auto background = [&]() {
        if (roleM) {
            if (!lo) {
                const int kq = wave - 4, o = (kq * 160 + (l >> 2)) * 7680 + 2560 + uid * 64 + (l & 3) * 16;
#pragma clang loop unroll(disable)
                for (int i = 0, j = 5 * (uid & 1); i < 10; ++i, j = j == 9 ? 0 : j + 1) dma16(rm, o, j * 16 * 7680, L_MT + kq * 10240 + j * 1024);
                DRAIN();
                f4 a0, a1; gemv_tile(L_MT + kq * 10240, L_S + kq * 320, l, a0, a1);
                if (g < 2) lds_st<float>(M_PART + l * 16 + kq * 4, g ? a1[0] : a0[0]);
            }
            BAR();
            if (wave == 4) {
                const float v0 = hsum(lds_ld<f4>(M_PART + c * 32)), v1 = hsum(lds_ld<f4>(M_PART + c * 32 + 16));
                st2<SC1>(u2{pkb(v0, v1), tag}, rw, WS_MODR + g * 10240 + uid * 128 + c * 8);
            }
        }
        if (roleF) {
            const int ft = 12 * x8 + r, kq = wave & 3, img = L_GU + wave * 5120;
            const rsrc_t rg = mkrs(lo ? wgate : wup, 640 * 3072);
            const int o = (kq * 160 + (l >> 1)) * 3072 + ft * 32 + (l & 1) * 16;
#pragma clang loop unroll(disable)
            for (int i = 0, j = r & 3; i < 5; ++i, j = j == 4 ? 0 : j + 1) dma16(rg, o, j * 32 * 3072, img + j * 1024);
            if (wave > 4) {
                const rsrc_t rd = mkrs(wdown + ft * 20480, 20480);
#pragma unroll
                for (int i = 0; i < 16; ++i) wdr[i] = ld2<0>(rd, hidx < 160 ? hidx * 8 : OOB, i * 1280);
            }
            DRAIN();
#pragma unroll
            for (int s = 0; s < 5; ++s) guf[s] = lds_tr2(img + s * 1024 + g * 256 + c * 8, img + s * 1024 + g * 256 + 128 + c * 8);
            if (wave > 4) {
#pragma unroll
                for (int i = 0; i < 16; i += 2) {
                    const u2 a = wdr[i], b = wdr[i + 1];
                    wdr[i] = u2{__builtin_amdgcn_perm(b[0], a[0], 0x05040100u), __builtin_amdgcn_perm(b[0], a[0], 0x07060302u)};
                    wdr[i + 1] = u2{__builtin_amdgcn_perm(b[1], a[1], 0x05040100u), __builtin_amdgcn_perm(b[1], a[1], 0x07060302u)};
                    PIN(wdr[i]); PIN(wdr[i + 1]);
                }
            }
        }
    };
    u4 c3[3], c3h[3];
    auto c3prep = [&]() {
#pragma unroll
        for (int T = 0; T < 3; ++T) {
            const float a40 = (float)(16 * T + c) * (1.f / 40.f), sc = (T < 2 || c < 8) ? 0.25f : 0.f;
            f2 v[6]; cis_run<6>(v, (float)(6 * g) * a40, a40, sc);
            const unsigned z = pkh(0.5f * sc, (c & 1) ? -0.5f * sc : 0.5f * sc), f = pkh(v[0][0], -v[0][1]);
            c3[T] = u4{g == 0 ? z : f, pkh(v[1][0], -v[1][1]), pkh(v[2][0], -v[2][1]), pkh(v[3][0], -v[3][1])};
            c3h[T] = u4{pkh(v[4][0], -v[4][1]), pkh(v[5][0], -v[5][1]), 0, 0};
            PIN(c3[T]); PIN(c3h[T]);
        }
    };
    u2 wrr[3];
    auto wrload = [&]() { const rsrc_t rb = mkrs(bwr, 1280); wrr[0] = ld2<0>(rb, ooff); wrr[1] = ld2<0>(rb, 512 + ooff); wrr[2] = ld2<0>(rb, o2); };
    f4 vga[3], vc1[3], vsb[3], vg1[3], vg0[3], vu[3]; float usum, rconst;
    auto modprep = [&]() {
        f4 a = zero4, b = zero4;
#pragma unroll
        for (int T = 0; T < 3; ++T) {
            const int o = L_MOD + T * 512 + ooff;
            const f4 wr = unb(wrr[T]), gb = unb(lds_ld<u2>(o + 3840));
            vga[T] = unb(lds_ld<u2>(o)) * 0.2f; vsb[T] = unb(lds_ld<u2>(o + 1280)); vc1[T] = unb(lds_ld<u2>(o + 2560)) + 1.f;
            vg1[T] = gb * vc1[T]; vg0[T] = gb * vsb[T] + m1; vu[T] = vc1[T] * wr;
            a += vu[T]; b += vsb[T] * wr;
        }
        const f2 a2 = lo2(a) + hi2(a), b2v = lo2(b) + hi2(b);
        const unsigned t = as_u(row_sum(fold4(a2[0], a2[1], b2v[0], b2v[1])));
        usum = as_f(rdl(t, 0)) + as_f(rdl(t, 16)); rconst = as_f(rdl(t, 32)) + as_f(rdl(t, 48));
#pragma unroll
        for (int T = 0; T < 3; ++T) { PIN(vga[T]); PIN(vsb[T]); PIN(vc1[T]); PIN(vg1[T]); PIN(vg0[T]); PIN(vu[T]); }
        PIN(usum); PIN(rconst);
    };

    if (lo) {
        u4 a1r[3][2], a1i[3][2], a1h[3], fre, fim; f4 atr[3], ati[3]; int xo[3];
        lds_st<u4>(L_CA + tid * 16, u4{SENT, SENT, SENT, SENT});
        if (tid < 32) lds_st<u4>(L_CA + 4096 + tid * 16, u4{SENT, SENT, SENT, SENT});
#pragma unroll
        for (int T = 0; T < 3; ++T) {
            const int cc = 16 * T + c;
            const float kap = (float)col_kappa(wave, cc);
            f2 v[10]; cis_run<10>(v, (float)g * kap * 0.25f, kap * (1.f / 40.f), 1.f);
#pragma unroll
            for (int i = 0; i < 10; ++i) {
                const unsigned re = pkh(v[i][0], v[i][1]), im = pkh(-v[i][1], v[i][0]);
                if (i < 8) { a1r[T][i >> 2][i & 3] = re; a1i[T][i >> 2][i & 3] = im; } else { a1h[T][i - 8] = re; a1h[T][i - 6] = im; }
            }
            const float ph = (float)(s2 * col_k1(wave, cc)) * (1.f / 2048.f), k640 = kap * (1.f / 640.f);
#pragma unroll
            for (int i = 0; i < 4; ++i) { const float th = -((float)(4 * g + i) * k640 + ph); atr[T][i] = cosr(th); ati[T][i] = sinr(th); }
            xo[T] = piece_off(wave, cc, g, s2);
        }
        if (wave == 0) {
            const unsigned sel = (c & 1) ? 0x01000c0cu : 0x0c0c0100u;
#pragma unroll
            for (int T = 0; T < 3; ++T)
#pragma unroll
                for (int i = 0; i < 4; ++i) {
                    a1r[T][0][i] = __builtin_amdgcn_perm(0u, a1r[T][0][i], sel); a1r[T][1][i] = __builtin_amdgcn_perm(0u, a1r[T][1][i], sel);
                    a1i[T][0][i] = __builtin_amdgcn_perm(0u, a1i[T][0][i], sel); a1i[T][1][i] = __builtin_amdgcn_perm(0u, a1i[T][1][i], sel);
                    a1h[T][i] = __builtin_amdgcn_perm(0u, a1h[T][i], sel);
                }
        }
#pragma unroll
        for (int i = 0; i < 4; ++i) { const f2 v = cis((float)((4 * g + i) * c) * (1.f / 16.f)) * 0.25f; fre[i] = pkh(v[0], v[1]); fim[i] = pkh(-v[1], v[0]); }
#pragma unroll
        for (int T = 0; T < 3; ++T) { PIN(a1r[T][0]); PIN(a1r[T][1]); PIN(a1i[T][0]); PIN(a1i[T][1]); PIN(a1h[T]); PIN(atr[T]); PIN(ati[T]); PIN(xo[T]); }
        PIN(fre); PIN(fim);
        constexpr unsigned P = 0x3800, M = 0xb800, p = 0x35a8, m = 0xb5a8;
        const bool odd = wave & 1; const unsigned pm = wave == 1 ? 0u : 0x80000000u;
        const u4 gr = odd ? u4{(P | p << 16) ^ pm, (m << 16) ^ pm, (M | m << 16) ^ pm, (p << 16) ^ pm} : (wave ? u4{P, M, P, M} : u4{P | P << 16, P | P << 16, P | P << 16, P | P << 16});
        const u4 gi = odd ? u4{m << 16, (M | m << 16) ^ (pm >> 16), p << 16, (P | p << 16) ^ (pm >> 16)} : (wave ? u4{M << 16, P << 16, M << 16, P << 16} : u4{P | M << 16, P | M << 16, P | M << 16, P | M << 16});
        TS(1);
        settag();
        u4 xp = {xpat(c & 3, 0), xpat(c & 3, 1), xpat(c & 3, 2), xpat(c & 3, 3)};
        u4 xpx = xp;
        if (c >= 8) { xpx[2] = xpat(2 * g + (c & 1), 2); xpx[3] = xpat(2 * g + (c & 1), 3); }
        int go = tid < 160 ? WS_CA + x8 * 10240 + tid * 16 : OOB;
        int cw = L_CA + (tid & 7) * 2 * CAP + ((tid >> 3) / 5) * 32 + ((tid >> 3) % 5) * 4, cr = L_CA + c * CAP + g * 32;
        PIN(xp); PIN(xpx); PIN(go); PIN(cw); PIN(cr);
        ln1();
        asm volatile("" :: "v"(cq));
        PRIO(2);
        nap<D_CA>();
        u4 gv[4];
        if (wave < 3) {
#pragma unroll
            for (int k = 0; k < 4; ++k) gv[k] = ld4<SC1>(rw, go, k * 2560);
        }
        unsigned y[10];
        {
            const int na = L_N + ((l >> 2) & 3) * NP + (l & 3) * 8 + 320 * g;
            u4 v[10];
#pragma unroll
            for (int i = 0; i < 10; ++i) v[i] = lds_tr2(na + 32 * i, na + 32 * i + 4 * NP);
            SB();
#pragma unroll
            for (int i = 0; i < 10; ++i) {
                float re = 0.f, im = 0.f;
#pragma unroll
                for (int k = 0; k < 4; ++k) { re = __builtin_amdgcn_fdot2(as_h2(v[i][k]), as_h2(gr[k]), re, false); im = __builtin_amdgcn_fdot2(as_h2(v[i][k]), as_h2(gi[k]), im, false); }
                y[i] = pkh(re, im);
            }
#pragma unroll
            for (int i = 0; i < 10; ++i) PIN(y[i]);
        }
        TS(4);
        unsigned addc = 0x30003000u;
#pragma clang loop unroll(disable)
        for (int pass = 0;; ++pass) {
            if (wave < 3) {
                for (int n = 0; n < SPIN; ++n) {
                    unsigned d = 0;
#pragma unroll
                    for (int k = 0; k < 4; ++k) d |= (gv[k][1] ^ tag) | (gv[k][3] ^ tag);
                    const bool bad = d != 0 && tid < 160;
                    if (!any(bad)) break;
                    FENCE();
                    if (bad) {
#pragma unroll
                        for (int k = 0; k < 4; ++k) gv[k] = ld4<SC1>(rw, go, k * 2560);
                    }
                }
                const h2 e8 = as_h2(0x30003000u);
                const h2 s0 = ((as_h2(gv[0][0]) + as_h2(gv[1][0])) + (as_h2(gv[2][0]) + as_h2(gv[3][0]))) * e8 + as_h2(addc);
                const h2 s1 = ((as_h2(gv[0][2]) + as_h2(gv[1][2])) + (as_h2(gv[2][2]) + as_h2(gv[3][2]))) * e8 + as_h2(addc);
                if (tid < 160) { lds_st<unsigned>(cw, as_u(s0)); lds_st<unsigned>(cw + CAP, as_u(s1)); }
            }
            if (pass == 0 || wave == 0) {
                u4 ca; unsigned cb;
                for (int n = 0; n < (SPIN << 4); ++n) {
                    FENCE();
                    ca = lds_ld<u4>(cr); cb = lds_ld<unsigned>(cr + 16);
                    if (!any(max(max(max(ca[0], ca[1]), ca[2]), max(ca[3], cb)) == SENT)) break;
                }
                TS(5);
                const unsigned cv[5] = {ca[0], ca[1], ca[2], ca[3], cb};
                unsigned z[10];
#pragma unroll
                for (int i = 0; i < 10; ++i) { const h2 t = as_h2(cv[i >> 1]); const _Float16 s = t[i & 1]; z[i] = as_u(as_h2(y[i]) * h2{s, s}); }
                const u4 d0 = {z[0], z[1], z[2], z[3]}, d1 = {z[4], z[5], z[6], z[7]}, d2r = {z[8], z[9], 0, 0}, d2i = {0, 0, z[8], z[9]};
                f4 re[3], im[3]; u4 uu[3];
#pragma unroll
                for (int T = 0; T < 3; ++T) {
                    re[T] = mfma(d2r, a1h[T], mfma(d1, a1r[T][1], mfma(d0, a1r[T][0], zero4)));
                    im[T] = mfma(d2i, a1h[T], mfma(d1, a1i[T][1], mfma(d0, a1i[T][0], zero4)));
                }
#pragma unroll
                for (int T = 0; T < 3; ++T) uu[T] = cmul_pack(re[T], im[T], atr[T], ati[T]);
#pragma unroll
                for (int T = 0; T < 3; ++T) { re[T] = mfma(fre, uu[T], zero4); im[T] = mfma(fim, uu[T], zero4); }
#pragma unroll
                for (int T = 0; T < 3; ++T) st4<SC1>((pack4(re[T], im[T]) & ~LSB2) | (T == 2 ? xpx : xp), rw, xo[T]);
            }
            if (w != W_SA || pass == 1) break;
            go = tid < 160 ? WS_SA + tid * 16 : OOB; cw += 2304; cr += 2304; addc = 0;
#pragma unroll
            for (int k = 0; k < 4; ++k) gv[k] = ld4<SC1>(rw, go, k * 2560);
#pragma unroll
            for (int i = 0; i < 10; ++i) y[i] = 0x3c00u;
#pragma unroll
            for (int T = 0; T < 3; ++T) xo[T] = piece_off(0, 16 * T + c, g, 256);
        }
        TS(6);
        PRIO(0);
        if (!roleB) BAR();
        f4 ctr[3], cti[3]; u4 c2r, c2i, c2r0, c2i0; int co[3];
#pragma unroll
        for (int i = 0; i < 3; ++i) {
#pragma unroll
            for (int e = 0; e < 4; ++e) {
                const int cc = 4 * (3 * wave + i) + e;
                const float th = (float)(c * col_kappa(g, cc)) * (1.f / 640.f) + (float)(s2 * col_k1(g, cc)) * (1.f / 2048.f);
                ctr[i][e] = cosr(th); cti[i][e] = sinr(th);
            }
            co[i] = piece_off(c >> 2, 4 * (3 * wave + i) + (c & 3), g, s2);
        }
        {
            const int s1 = c >> 1, h = c & 1;
            const float th = (float)(s1 * g) * 0.125f, cs = 0.5f * cosr(th), si = 0.5f * sinr(th), sg = (s1 & 1) ? -0.5f : 0.5f;
            const unsigned r0 = pkh(cs, -si), i0 = pkh(si, cs), r1 = g ? r0 : pkh(sg, 0.f), i1 = g ? pkh(-si, -cs) : pkh(0.f, sg);
            c2r = h ? u4{0, 0, r0, r1} : u4{r0, r1, 0, 0}; c2i = h ? u4{0, 0, i0, i1} : u4{i0, i1, 0, 0};
            const unsigned d2 = pkh(2.f * cs, -2.f * si);
            c2r0 = c2r; c2i0 = c2i;
            if (wave == 0 && !h) { c2r0[0] = g ? d2 : r0; c2r0[1] = g ? 0u : r1; c2i0[0] = 0u; c2i0[1] = g ? d2 : 0u; }
        }
        u4 gre = fre ^ 0x80000000u, gim = fim ^ 0x00008000u;
        const unsigned xq = xp0 ^ 0x10000u;
        unsigned m1v = co[1] == OOB ? 0u : LSB2, m2v = co[2] == OOB ? 0u : LSB2;
        int e3w = L_E3 + (32 * g + c) * E3P + wave * 32;
#pragma unroll
        for (int i = 0; i < 3; ++i) { PIN(ctr[i]); PIN(cti[i]); PIN(co[i]); }
        PIN(c2r); PIN(c2i); PIN(c2r0); PIN(c2i0); PIN(gre); PIN(gim); PIN(m1v); PIN(m2v); PIN(e3w);
        TS(7);
        if (roleB) {
            int so = WS_X + seg * XSTR + (64 * g + 4 * wave) * 64 + c * 16;
            int ea = L_E1 + c * E1P + (16 * (g >> 1) + 4 * wave + 2 * (g & 1)) * 32, eb = L_E2 + wave * E2P + l * 8;
            PIN(so); PIN(ea); PIN(eb);
            nap<D_X1>();
            PRIO(2);
            u4 v[4];
            TS(8);
#pragma unroll
            for (int i = 0; i < 4; ++i) v[i] = ld4<SC1>(rw, so + i * 1024);
            PRIO(0);
            c3prep();
            PRIO(2);
            for (int n = 0; n < SPIN; ++n) {
                const u4 d = ((v[0] ^ xp) | (v[1] ^ xp)) | ((v[2] ^ xp) | (v[3] ^ xp));
                unsigned b = ((d[0] | d[1]) | (d[2] | d[3])) & LSB2;
                if (!any(b != 0)) break;
                b |= dpp<0xb1>(b); b |= dpp<0x4e>(b);
                FENCE();
                if (b) {
#pragma unroll
                    for (int i = 0; i < 4; ++i) v[i] = ld4<SC1>(rw, so + i * 1024);
                }
            }
            TS(9);
            f4 re[4], im[4];
#pragma unroll
            for (int mu = 0; mu < 4; ++mu) {
                const u4 d = {v[0][mu], v[1][mu], v[2][mu], v[3][mu]};
                re[mu] = mfma(d, fre, zero4); im[mu] = mfma(d, fim, zero4);
            }
#pragma unroll
            for (int h = 0; h < 2; ++h) {
                const u2 a = pkh4(re[2 * h]), b = pkh4(re[2 * h + 1]), cc = pkh4(im[2 * h]), d = pkh4(im[2 * h + 1]);
                lds_st<u4>(ea + 16 * h, u4{a[0], a[1], b[0], b[1]}); lds_st<u4>(ea + 32 + 16 * h, u4{cc[0], cc[1], d[0], d[1]});
            }
            BAR();
            BAR();
            u4 d[4];
#pragma unroll
            for (int mu = 0; mu < 4; ++mu) d[mu] = lds_tr2(eb + mu * 1024, eb + mu * 1024 + 512);
            SB();
#pragma unroll
            for (int mu = 0; mu < 4; ++mu) { re[mu] = mfma(gre, d[mu], zero4); im[mu] = mfma(gim, d[mu], zero4); }
#pragma unroll
            for (int i = 0; i < 4; ++i)
                st4<SC1>(u4{(pkh(re[0][i], im[0][i]) & ~LSB2) | xq, pkh(re[1][i], im[1][i]), pkh(re[2][i], im[2][i]), pkh(re[3][i], im[3][i])}, rw, so + i * 1024);
            PRIO(0);
            TS(10);
        } else c3prep();
        wrload();
        background();
        BAR();
        TS(11);
        if (roleB) nap<D_X2>();
        else {
            for (int n = 0; n < SPIN; ++n) { FENCE(); if (!any(((ld1<SC1>(rw, WS_X + (20 * x8 + r) * XSTR + s2 * 64) ^ xq) & LSB2) != 0)) break; }
            nap<D_CAN>();
        }
        u4 v[3];
        TS(12);
#pragma unroll
        for (int i = 0; i < 3; ++i) v[i] = ld4<SC1>(rw, co[i]);
        modprep();
        PRIO(2);
        for (int n = 0; n < SPIN; ++n) {
            const bool bad = (((v[0][0] ^ xq) & LSB2) | ((v[1][0] ^ xq) & m1v) | ((v[2][0] ^ xq) & m2v)) != 0;
            if (!any(bad)) break;
            FENCE();
            if (bad) {
#pragma unroll
                for (int i = 0; i < 3; ++i) v[i] = ld4<SC1>(rw, co[i]);
            }
        }
        TS(13);
        f4 re[3], im[3]; u4 t[3];
#pragma unroll
        for (int i = 0; i < 3; ++i) { re[i] = mfma(v[i], gre, zero4); im[i] = mfma(v[i], gim, zero4); }
#pragma unroll
        for (int i = 0; i < 3; ++i) t[i] = cmul_pack(re[i], im[i], ctr[i], cti[i]);
#pragma unroll
        for (int i = 0; i < 3; ++i) { re[i] = mfma(i ? c2r : c2r0, t[i], zero4); im[i] = mfma(i ? c2i : c2i0, t[i], zero4); }
#pragma unroll
        for (int i = 0; i < 3; ++i) t[i] = pack4(re[i], im[i]);
        lds_st<u4>(e3w, u4{t[0][0], t[0][1], t[1][0], t[1][1]}); lds_st<u4>(e3w + 16 * E3P, u4{t[0][2], t[0][3], t[1][2], t[1][3]});
        lds_st<u2>(e3w + 16, u2{t[2][0], t[2][1]}); lds_st<u2>(e3w + 16 * E3P + 16, u2{t[2][2], t[2][3]});
        PRIO(0);
    } else {
        if (urgent) asm volatile("s_waitcnt vmcnt(14)" ::: "memory"); else asm volatile("s_waitcnt vmcnt(4)" ::: "memory");
        u2 cq2; asm volatile("v_mov_b64 %0, %1" : "=v"(cq2) : "v"(cq));
        const f4 cv = unb(cq2);
        lds_st<u2>(wave == 4 ? L_SU + l * 8 : L_S + hidx * 8, u2{pkb(silu(cv[0]), silu(cv[1])), pkb(silu(cv[2]), silu(cv[3]))});
        if (wave > 4) {
            const int q = x8 >> 1, ri = hidx / 6, ln = hidx % 6, cls = q == 0 ? (ri >> 3) * 4 : ((x8 & 1) ? 8 - q : q);
            const int o = ri < (q == 0 ? 16 : 8) ? ((cls + 8 * (8 * r + (ri & 7))) * 642 + ln * 128) & ~3 : OOB;
            if (wave < 7) { dma4(mkrs(awr, 2048 * 642), o, L_SINK); dma4(mkrs(awi, 2048 * 642), o, L_SINK); }
        }
        settag();
        if (urgent) {
            asm volatile("s_waitcnt vmcnt(3)" ::: "memory");
            TS(24);
            f4 a0, a1; gemv_tile(L_UT, L_SU, l, a0, a1);
            const u2 gr = {pkh(a0[0], a1[0]), tag};
            const int o = (r < 10 ? WS_CA : WS_SA) + g * 10240 + (ukq * 20 + ucg) * 128 + c * 8;
            st2<SC1>(gr, rw, o); st2<SC1>(gr, rw, o, 40960);
            TS(1);
        }
        PRIO(1);
        ln1();
        PRIO(0);
        if (roleB) {
            const int wq = wave - 4, e = c & 3, mulo = c >> 2, cc = 4 * bu + e;
            const int k1 = col_k1(bq, cc), kd = bu == 10 ? 20 + 40 * (4 * (e >> 1) + mulo) : col_kappa(bq, cc) + 40 * (4 * bg + mulo);
            const bool edge = kd == 0 || kd == 320;
            const int col = bg < 2 ? kd : 640 - kd, ks0 = k1 + 8 * wq + 512 * g;
            const rsrc_t rr = mkrs(awr, 2048 * 642), ri = mkrs(awi, 2048 * 642);
            f4 wr[4], wi[4];
#pragma unroll
            for (int j = 0; j < 4; ++j) { wr[j] = zero4; wi[j] = zero4; }
            const int np = (bu == 0 && !(bg & 1)) ? 2 : 1;
            nap<D_W>();
            TS(5);
#pragma clang loop unroll(disable)
            for (int ps = 0; ps < np; ++ps) {
                const bool mir = (bg >= 2) != (ps == 1), off = ps && !edge;
                const float sr = edge ? 0.5f : 1.f, si = mir ? -sr : sr;
                const int vo = off ? OOB : ((mir ? 1568 - ks0 : ks0) * 321 + col) * 2, v0 = off ? OOB : (((mir ? -ks0 : ks0) & 2047) * 321 + col) * 2;
                unsigned short tr[16], ti[16];
#pragma unroll
                for (int n = 0; n < 16; ++n) {
                    const int so = n ? 32 * 642 * (mir ? 15 - n : n) : 0;
                    tr[n] = __builtin_amdgcn_raw_buffer_load_b16(rr, n ? vo : v0, so, 0); ti[n] = __builtin_amdgcn_raw_buffer_load_b16(ri, n ? vo : v0, so, 0);
                }
                SB();
#pragma unroll
                for (int n = 0; n < 16; ++n) { wr[n & 3][n >> 2] += sr * bflo(tr[n]); wi[n & 3][n >> 2] += si * bflo(ti[n]); }
            }
            TS(6);
            u4 b2r[4], b2i[4], b3r[4], b3i[4];
            f2 v3[16]; cis_run<16>(v3, (float)(c * (wq + 64 * g)) * (1.f / 256.f), (float)c * (1.f / 64.f), 0.25f);
#pragma unroll
            for (int j = 0; j < 4; ++j)
#pragma unroll
                for (int i = 0; i < 4; ++i) {
                    const f2 v2 = cis((float)(4 * g + i) * ((float)(wq + 4 * j + 16 * c) * (1.f / 256.f)));
                    b2r[j][i] = pkh(v2[0], v2[1]); b2i[j][i] = pkh(-v2[1], v2[0]);
                    b3r[j][i] = pkh(v3[j + 4 * i][0], -v3[j + 4 * i][1]); b3i[j][i] = pkh(v3[j + 4 * i][1], v3[j + 4 * i][0]);
                }
            int ra = L_E1 + wq * E1P + l * 8, wa = L_E2 + (c >> 2) * E2P + g * 1024 + (16 * (wq >> 1) + 2 * (wq & 1)) * 32 + (c & 3) * 8;
#pragma unroll
            for (int j = 0; j < 4; ++j) { PIN(b2r[j]); PIN(b2i[j]); PIN(b3r[j]); PIN(b3i[j]); PIN(wr[j]); PIN(wi[j]); }
            f4 sar = zero4, sai = zero4;
            if (bq == 0 && wave == 4) {
                const unsigned pt = xpat(e, mulo);
                unsigned t;
                for (int n = 0; n < SPIN; ++n) { FENCE(); t = ld1<SC1>(rw, WS_X + seg * XSTR + 256 * 64 + e * 16 + mulo * 4); if (!any(((t ^ pt) & LSB2) != 0)) break; }
                const h2 th = as_h2(t);
                if (g == 0 && k1 == 0) { sar[0] = 256.f * (float)th[0]; sai[0] = 256.f * (float)th[1]; }
            }
            PIN(sar); PIN(sai); PIN(ra); PIN(wa);
            PRIO(2);
            TS(7);
            BAR();
            TS(8);
            u4 d[4]; f4 re[4], im[4];
#pragma unroll
            for (int j = 0; j < 4; ++j) d[j] = lds_tr2(ra + 4 * j * E1P, ra + 4 * j * E1P + 512);
            SB();
#pragma unroll
            for (int j = 0; j < 4; ++j) { re[j] = mfma(b2r[j], d[j], j ? zero4 : sar); im[j] = mfma(b2i[j], d[j], j ? zero4 : sai); }
#pragma unroll
            for (int j = 0; j < 4; ++j) d[j] = cmul_pack(re[j], im[j], wr[j], wi[j]);
#pragma unroll
            for (int j = 0; j < 4; ++j) { re[j] = mfma(d[j], b3r[j], zero4); im[j] = mfma(d[j], b3i[j], zero4); }
#pragma unroll
            for (int j = 0; j < 4; ++j) { lds_st<u2>(wa + 128 * j, pkh4(re[j])); lds_st<u2>(wa + 128 * j + 32, pkh4(im[j])); }
            BAR();
            PRIO(0);
            TS(9);
            if (w == 255 && wave == 4) st1<SC1>(tag, rw, l == 0 ? WS_EPOCH : OOB);
        } else BAR();
        wrload();
        background();
        {
            const int i0 = tid - 256, o = WS_MODR + (x8 & 3) * 10240 + i0 * 16;
            u4 v[3];
            for (int n = 0; n < SPIN; ++n) {
                FENCE();
                v[0] = ld4<SC1>(rw, o); v[1] = ld4<SC1>(rw, o, 4096); v[2] = ld4<SC1>(rw, wave < 6 ? o : o - 2048, 8192);
                unsigned d = 0;
#pragma unroll
                for (int k = 0; k < 3; ++k) d |= (v[k][1] ^ tag) | (v[k][3] ^ tag);
                if (!any(d != 0)) break;
            }
#pragma unroll
            for (int k = 0; k < 3; ++k) lds_st<u2>(L_MOD + k * 2048 + i0 * 8, u2{v[k][0], v[k][2]});
        }
        TS(10);
        BAR();
        c3prep();
        modprep();
        TS(11);
    }
    int e3r = L_E3 + (16 * wave + c) * E3P + g * 32, ko = l < 8 ? WS_KEY + l * 4096 + w * 16 : OOB;
    int sw_ = M_STAT + wave * 16 + g * 4, rid = 2047 - (256 * (l & 7) + s2);
    PIN(e3r); PIN(ko); PIN(sw_); PIN(rid);
    BAR();
    TS(14);

    {
        const u4 d = lds_ld<u4>(e3r); const u2 dh = lds_ld<u2>(e3r + 16);
        SB();
        f4 y[3];
#pragma unroll
        for (int T = 0; T < 3; ++T) y[T] = mfma(u4{dh[0], dh[1], 0, 0}, c3h[T], mfma(d, c3[T], zero4));
#pragma unroll
        for (int T = 0; T < 3; ++T) xc[T] += vga[T] * y[T];
        const f4 t = xc[0] + xc[1] + xc[2];
        f2 q = lo2(xc[0]) * lo2(xc[0]), u = lo2(xc[0]) * lo2(vu[0]);
        q += hi2(xc[0]) * hi2(xc[0]); u += hi2(xc[0]) * hi2(vu[0]);
#pragma unroll
        for (int T = 1; T < 3; ++T) { q += lo2(xc[T]) * lo2(xc[T]); u += lo2(xc[T]) * lo2(vu[T]); q += hi2(xc[T]) * hi2(xc[T]); u += hi2(xc[T]) * hi2(vu[T]); }
        lds_st<float>(sw_, row_sum(fold4(hsum(t), q[0] + q[1], u[0], u[1])));
    }
    BAR();
    const f4 st = lds_ld<f4>(M_STAT + (l & 7) * 16);
    const float m2l = st[0] * (1.f / 640.f);
    const float rsl = __builtin_amdgcn_rsqf(fmaxf(st[1] * (1.f / 640.f) - m2l * m2l, 0.f) + 1e-5f);
    const unsigned sbits = as_u(rsl * ((st[2] + st[3]) - m2l * usum) + rconst);
    const unsigned keyl = ((sbits ^ ((unsigned)((int)sbits >> 31) | 0x80000000u)) & 0xfffff800u) | (unsigned)rid;
    unsigned kmax = max(keyl, dpp<0x124>(keyl)); kmax = max(kmax, dpp<0x122>(kmax)); kmax = max(kmax, dpp<0x121>(kmax));
    if (wave == 0) st2<SC1>(u2{kmax, tag}, rw, ko);
    TS(15);
    const unsigned key = rdl(kmax, 0);
    const bool cand = (int)((2047 - (key & 2047)) >> 8) == wave;
    const float m2 = as_f(rdl(as_u(m2l), wave)), rs2 = as_f(rdl(as_u(rsl), wave));
    const rsrc_t ro = mkrs(out, 2048 * 1280);
    u2 ov[3]; f4 z[3];
#pragma unroll
    for (int T = 0; T < 3; ++T) z[T] = xc[T] * rs2 - m2 * rs2;
    if (cand) {
        PRIO(2);
        const u2 h0 = pkb4(z[0] * vc1[0] + vsb[0]), h1 = pkb4(z[1] * vc1[1] + vsb[1]), h2v = pkb4(z[2] * vc1[2] + vsb[2]);
        st4<SC1>(u4{h0[0], h0[1], h1[0], tag}, rw, WS_ROW + w * 2048 + l * 16);
        st4<SC1>(u4{h1[1], h2v[0], h2v[1], tag}, rw, WS_ROW + w * 2048 + 1024 + l * 16);
    }
#pragma unroll
    for (int T = 0; T < 3; ++T) ov[T] = pkb4(xc[T] + (z[T] * vg1[T] + vg0[T]));
    bool wr_out = true;
    if (cand) {
        PIN(ov[0]); PIN(ov[1]); PIN(ov[2]);
        if (roleF) nap<D_KEY>(); else nap<D_KEYB>();
        const int o = WS_KEY + x8 * 4096 + l * 16;
        u2 k[4];
        TS(16);
        for (int n = 0; n < SPIN; ++n) {
            FENCE();
#pragma unroll
            for (int i = 0; i < 4; ++i) k[i] = ld2<SC1>(rw, o + i * 1024);
            if (!any((((k[0][1] ^ tag) | (k[1][1] ^ tag)) | ((k[2][1] ^ tag) | (k[3][1] ^ tag))) != 0)) break;
        }
        const unsigned win = wave_umax(max(max(k[0][0], k[1][0]), max(k[2][0], k[3][0])));
        TS(17);
        wr_out = win != key;
        if (roleF) {
            const int sw = (2047 - (win & 2047)) & 255, ro2 = WS_ROW + ((sw & 31) * 8 + (sw >> 5)) * 2048;
            u4 v0, v1;
            for (int n = 0; n < SPIN; ++n) {
                FENCE();
                v0 = ld4<SC1>(rw, l * 16, ro2); v1 = ld4<SC1>(rw, l * 16 + 1024, ro2);
                if (!any(((v0[3] ^ tag) | (v1[3] ^ tag)) != 0)) break;
            }
            lds_st<u2>(L_H + ooff, u2{v0[0], v0[1]}); lds_st<u2>(L_H + 512 + ooff, u2{v0[2], v1[0]}); lds_st<u2>(L_H + 1024 + ooff, u2{v1[1], v1[2]});
            lds_st<unsigned>(M_WIN, win);
            TS(18);
        }
    }
    if (!roleF || !cand) {
        if (wr_out) { st2<0>(ov[0], ro, ooff, rowb); st2<0>(ov[1], ro, 512 + ooff, rowb); st2<0>(ov[2], ro, o2, rowb); }
        else {
#pragma unroll
            for (int T = 0; T < 3; ++T) st4<SC1>(u4{ov[T][0], tag, ov[T][1], tag}, rw, WS_BASE + T * 1024 + 2 * ooff);
        }
    }
    if (!roleF) { TSDUMP(); return; }

    {
        int ha = L_H + (wave & 3) * 320 + g * 16, pa = M_PART + (wave >> 2) * 256 + c * 16 + (wave & 3) * 4;
        PIN(ha); PIN(pa);
        BAR();
        u4 hf[5];
#pragma unroll
        for (int s = 0; s < 5; ++s) hf[s] = lds_ld<u4>(ha + s * 64);
        SB();
        f4 a = zero4;
#pragma unroll
        for (int s = 0; s < 5; ++s) a = mfmab(hf[s], guf[s], a);
        lds_st<float>(pa, a[0]);
    }
    BAR();
    TS(19);
    const unsigned ytag = (tag >> (8 * (l & 3))) & 0xffu;
    if (wave > 4) {
        const float act = silu(hsum(lds_ld<f4>(M_PART + c * 16))) * hsum(lds_ld<f4>(M_PART + 256 + c * 16));
        const unsigned pr = pkb(act, dppf<0x101>(act));
        f4 a = zero4;
#pragma unroll
        for (int i = 0; i < 8; ++i) {
            const b2 s = as_b2(rdl(pr, 2 * i));
            const u2 p0 = wdr[2 * i], p1 = wdr[2 * i + 1];
            a[0] = __builtin_amdgcn_fdot2_f32_bf16(as_b2(p0[0]), s, a[0], false); a[1] = __builtin_amdgcn_fdot2_f32_bf16(as_b2(p0[1]), s, a[1], false);
            a[2] = __builtin_amdgcn_fdot2_f32_bf16(as_b2(p1[0]), s, a[2], false); a[3] = __builtin_amdgcn_fdot2_f32_bf16(as_b2(p1[1]), s, a[3], false);
        }
        st4<SC1>(u4{(as_u(a[0]) & ~0xffu) | ytag, as_u(a[1]), as_u(a[2]), as_u(a[3])}, rw, hidx < 160 ? WS_YP + (12 * x8 + r) * 2560 + hidx * 16 : OOB);
        TS(20);
    }
    if (cand) {
        if (wr_out) { st2<0>(ov[0], ro, ooff, rowb); st2<0>(ov[1], ro, 512 + ooff, rowb); st2<0>(ov[2], ro, o2, rowb); }
        else {
#pragma unroll
            for (int T = 0; T < 3; ++T) st4<SC1>(u4{ov[T][0], tag, ov[T][1], tag}, rw, WS_BASE + T * 1024 + 2 * ooff);
        }
    }
    if (wave == 4 && r < 5) {
        const unsigned win = (unsigned)__builtin_amdgcn_readfirstlane((int)lds_ld<unsigned>(M_WIN));
        const int row = 2047 - (win & 2047), col = 16 * (5 * x8 + r) + 4 * (l & 3) + g;
        const unsigned kb = (win & 0xfffff800u) | 0x400u;
        const float sc = as_f(kb ^ ((kb >> 31) ? 0x80000000u : 0xffffffffu));
        float gs = bflo(lds_ld<unsigned short>(L_MOD + 3840 + col * 2)) * __builtin_amdgcn_rcpf(1.f + __expf(-sc));
        int yo = WS_YP + (l >> 2) * 2560 + (5 * x8 + r) * 64 + (l & 3) * 16, bo = WS_BASE + (col >> 1) * 8, oo = c < 4 ? row * 1280 + col * 2 : OOB;
        PIN(gs); PIN(yo); PIN(bo); PIN(oo);
        nap<D_YP>();
        u4 v[6]; u2 bs;
        TS(20);
        for (int n = 0; n < SPIN; ++n) {
            FENCE();
#pragma unroll
            for (int i = 0; i < 6; ++i) v[i] = ld4<SC1>(rw, yo, i * 40960);
            bs = ld2<SC1>(rw, bo);
            unsigned d = 0;
#pragma unroll
            for (int i = 0; i < 6; ++i) d |= v[i][0] ^ ytag;
            if (!any(((d & 0xffu) | (bs[1] ^ tag)) != 0)) break;
        }
        TS(21);
        const f4 a = ((as_f4(v[0]) + as_f4(v[1])) + (as_f4(v[2]) + as_f4(v[3]))) + (as_f4(v[4]) + as_f4(v[5]));
        float t = fold4(a[0], a[1], a[2], a[3]);
        t += dppf<0x124>(t); t += dppf<0x128>(t);
        const float base = (g & 1) ? bfhi(bs[0]) : bflo(bs[0]);
        __builtin_amdgcn_raw_buffer_store_b16((unsigned short)pkb(base + gs * t, 0.f), ro, oo, 0, 0);
        TS(22);
        asm volatile(".Lgf_end:");
    }
    TSDUMP();
}

extern "C" void kernel_launch(void* const* d_in, const int* in_sizes, int n_in, void* d_out, int out_size, void* d_ws, size_t ws_size, hipStream_t stream) {
    gfnet_mod_kernel<<<256, 512, 0, stream>>>((const char*)d_in[0], (const char*)d_in[9], (const char*)d_in[8], (char*)d_ws, (const char*)d_in[1], (const char*)d_in[2],
                                              (const char*)d_in[4], (char*)d_out, (const char*)d_in[5], (const char*)d_in[6], (const char*)d_in[7]);
}
```

```cpp
#define STAMP 1
#define AMP_IDX 1
#define AMP_MASK 16
#define AMP_MEAN 0
#include <hip/hip_runtime.h>
#include <stdint.h>
#pragma clang diagnostic ignored "-Wint-to-pointer-cast"

typedef _Float16 h2 __attribute__((ext_vector_type(2)));
typedef _Float16 h8 __attribute__((ext_vector_type(8)));
typedef __bf16 b2 __attribute__((ext_vector_type(2)));
typedef __bf16 b8 __attribute__((ext_vector_type(8)));
typedef short s4 __attribute__((ext_vector_type(4)));
typedef float f2 __attribute__((ext_vector_type(2)));
typedef float f4 __attribute__((ext_vector_type(4)));
typedef unsigned u2 __attribute__((ext_vector_type(2)));
typedef unsigned u4 __attribute__((ext_vector_type(4)));
typedef unsigned long long ul2 __attribute__((ext_vector_type(2)));
typedef unsigned long long ul4 __attribute__((ext_vector_type(4)));
typedef __amdgpu_buffer_rsrc_t rsrc_t;
#define LAS __attribute__((address_space(3)))
#define DEV __device__ __forceinline__

#define D_CA 0
#define D_W 2
#define D_X1 0
#define D_X2 0
#define D_KEY 2
#define D_KEYB 10
#define D_YP 8
#define D_CAN 0
#define PF_WG 4

constexpr int WS_EPOCH = 0;
constexpr int WS_BASE = 1024;
constexpr int WS_KEY = 4096;
constexpr int WS_CA = WS_KEY + 32768;
constexpr int WS_SA = WS_CA + 81920;
constexpr int WS_MODR = WS_SA + 81920;
constexpr int WS_YP = 262144;
constexpr int WS_ROW = WS_YP + 245760;
constexpr int XSTR = 16640;
constexpr int WS_X = 1 << 20;
constexpr int WS_END = WS_X + 161 * XSTR;
constexpr int WS_DBG = 8 << 20;
static_assert(WS_MODR + 40960 <= WS_YP && WS_ROW + 524288 <= WS_X && WS_END < WS_DBG, "workspace map");

constexpr int L_N = 0, NP = 1312;
constexpr int L_CA = 10496, CAP = 144;
constexpr int L_S = L_CA + 2 * 2304;
constexpr int L_SU = L_S + 1536;
constexpr int L_MISC = L_SU + 512;
constexpr int L_H = L_MISC + 1024;
constexpr int L_MOD = L_H + 1536;
constexpr int L_E3 = L_MOD + 6144, E3P = 144;
constexpr int L_UT = L_E3 + 128 * E3P;
constexpr int L_SINK = L_UT + 10240;
constexpr int L_E1 = L_SINK + 256, E1P = 1040;
constexpr int L_E2 = L_E1 + 16640, E2P = 4128;
constexpr int L_MT = L_E1;
constexpr int L_GU = L_MT + 40960;
constexpr int L_END = L_GU + 40960;
static_assert(L_E2 + 4 * E2P <= L_GU && L_E1 % 16 == 0 && L_E3 % 16 == 0 && L_END + 2048 <= 163840, "LDS map");
constexpr int M_STAT = L_MISC, M_WIN = L_MISC + 128, M_PART = L_MISC + 256;

constexpr int OOB = 0x7ffffff0;
constexpr unsigned SENT = 0xffffffffu, LSB2 = 0x00010001u;
constexpr int SPIN = 1 << 15;
constexpr int SC1 = 16;
constexpr int W_DUAL = 88, W_SA = 89;

DEV h2 as_h2(unsigned v) { return __builtin_bit_cast(h2, v); }
DEV b2 as_b2(unsigned v) { return __builtin_bit_cast(b2, v); }
DEV unsigned as_u(h2 v) { return __builtin_bit_cast(unsigned, v); }
DEV unsigned as_u(float v) { return __builtin_bit_cast(unsigned, v); }
DEV float as_f(unsigned v) { return __builtin_bit_cast(float, v); }
DEV f4 as_f4(u4 v) { return __builtin_bit_cast(f4, v); }
DEV unsigned pkh(float a, float b) { f2 p = {a, b}; return as_u(__builtin_convertvector(p, h2)); }
DEV unsigned pkb(float a, float b) { f2 p = {a, b}; return __builtin_bit_cast(unsigned, __builtin_convertvector(p, b2)); }
DEV float bflo(unsigned v) { return as_f(v << 16); }
DEV float bfhi(unsigned v) { return as_f(v & 0xffff0000u); }
DEV f4 unb(u2 v) { const unsigned a = v[0], b = v[1]; f4 r = {bflo(a), bfhi(a), bflo(b), bfhi(b)}; return r; }
DEV u2 pkb4(f4 v) { return u2{pkb(v[0], v[1]), pkb(v[2], v[3])}; }
DEV u2 pkh4(f4 v) { return u2{pkh(v[0], v[1]), pkh(v[2], v[3])}; }
DEV f4 mfma(u4 a, u4 b, f4 c) { return __builtin_amdgcn_mfma_f32_16x16x32_f16(__builtin_bit_cast(h8, a), __builtin_bit_cast(h8, b), c, 0, 0, 0); }
DEV f4 mfmab(u4 a, u4 b, f4 c) { return __builtin_amdgcn_mfma_f32_16x16x32_bf16(__builtin_bit_cast(b8, a), __builtin_bit_cast(b8, b), c, 0, 0, 0); }
DEV u4 pack4(f4 re, f4 im) { u4 r = {pkh(re[0], im[0]), pkh(re[1], im[1]), pkh(re[2], im[2]), pkh(re[3], im[3])}; return r; }
DEV u4 cmul_pack(f4 re, f4 im, f4 wr, f4 wi) { return pack4(re * wr - im * wi, re * wi + im * wr); }
DEV rsrc_t mkrs(const void* p, int n) { return __builtin_amdgcn_make_buffer_rsrc((void*)p, 0, n, 0x00020000); }
template <int AUX> DEV u4 ld4(rsrc_t r, int o, int so = 0) { return __builtin_bit_cast(u4, __builtin_amdgcn_raw_buffer_load_b128(r, o, so, AUX)); }
template <int AUX> DEV u2 ld2(rsrc_t r, int o, int so = 0) { return __builtin_bit_cast(u2, __builtin_amdgcn_raw_buffer_load_b64(r, o, so, AUX)); }
template <int AUX> DEV unsigned ld1(rsrc_t r, int o) { return __builtin_amdgcn_raw_buffer_load_b32(r, o, 0, AUX); }
template <int AUX> DEV void st4(u4 v, rsrc_t r, int o) { __builtin_amdgcn_raw_buffer_store_b128(v, r, o, 0, AUX); }
template <int AUX> DEV void st2(u2 v, rsrc_t r, int o, int so = 0) { __builtin_amdgcn_raw_buffer_store_b64(v, r, o, so, AUX); }
template <int AUX> DEV void st1(unsigned v, rsrc_t r, int o) { __builtin_amdgcn_raw_buffer_store_b32(v, r, o, 0, AUX); }
template <class T> DEV T lds_ld(int a) { return *(const LAS T*)a; }
template <class T> DEV void lds_st(int a, T v) { *(LAS T*)a = v; }
DEV u2 lds_tr(int a) { return __builtin_bit_cast(u2, __builtin_amdgcn_ds_read_tr16_b64_v4i16((LAS s4*)a)); }
DEV u4 lds_tr2(int a0, int a1) { const u2 x = lds_tr(a0), y = lds_tr(a1); u4 r = {x[0], x[1], y[0], y[1]}; return r; }
DEV void dma16(rsrc_t r, int voff, int soff, int ldsbase) { asm volatile("s_mov_b32 m0, %3\n\ts_nop 0\n\tbuffer_load_dwordx4 %0, %1, %2 offen lds" :: "v"(voff), "s"(r), "s"(soff), "s"(ldsbase) : "memory"); }
DEV void dma4(rsrc_t r, int voff, int ldsbase) { asm volatile("s_mov_b32 m0, %2\n\ts_nop 0\n\tbuffer_load_dword %0, %1, 0 offen lds" :: "v"(voff), "s"(r), "s"(ldsbase) : "memory"); }
#define DRAIN() asm volatile("s_waitcnt vmcnt(0)" ::: "memory")
#define BAR() asm volatile("s_waitcnt lgkmcnt(0)\n\ts_barrier" ::: "memory")
#define FENCE() asm volatile("" ::: "memory")
#define SB() __builtin_amdgcn_sched_barrier(0)
#define PIN(x) asm volatile("" : "+v"(x))
#define PRIO(n) __builtin_amdgcn_s_setprio(n)
template <int N> DEV void nap() { if (N > 0) __builtin_amdgcn_s_sleep(N); }
DEV bool any(bool b) { return __builtin_amdgcn_ballot_w64(b) != 0; }
template <int CTRL> DEV unsigned dpp(unsigned v) { return (unsigned)__builtin_amdgcn_update_dpp(0, (int)v, CTRL, 0xf, 0xf, true); }
template <int CTRL> DEV float dppf(float v) { return as_f(dpp<CTRL>(as_u(v))); }
DEV unsigned rdl(unsigned v, int lane) { return (unsigned)__builtin_amdgcn_readlane((int)v, lane); }
DEV float row_sum(float v) { v += dppf<0x128>(v); v += dppf<0x124>(v); v += dppf<0x122>(v); v += dppf<0x121>(v); return v; }
DEV float fold4(float a, float b, float c, float d) {
    const auto p = __builtin_amdgcn_permlane32_swap(as_u(a), as_u(c), false, false);
    const auto q = __builtin_amdgcn_permlane32_swap(as_u(b), as_u(d), false, false);
    const auto s = __builtin_amdgcn_permlane16_swap(as_u(as_f(p[0]) + as_f(p[1])), as_u(as_f(q[0]) + as_f(q[1])), false, false);
    return as_f(s[0]) + as_f(s[1]);
}
DEV float wave_sum(float v) {
    v = row_sum(v);
    return (as_f(rdl(as_u(v), 0)) + as_f(rdl(as_u(v), 16))) + (as_f(rdl(as_u(v), 32)) + as_f(rdl(as_u(v), 48)));
}
DEV unsigned wave_umax(unsigned v) {
    v = max(v, dpp<0x128>(v)); v = max(v, dpp<0x124>(v)); v = max(v, dpp<0x122>(v)); v = max(v, dpp<0x121>(v));
    return max(max(rdl(v, 0), rdl(v, 16)), max(rdl(v, 32), rdl(v, 48)));
}
DEV f2 lo2(f4 v) { return f2{v[0], v[1]}; }
DEV f2 hi2(f4 v) { return f2{v[2], v[3]}; }
DEV float hsum(f4 v) { const f2 t = lo2(v) + hi2(v); return t[0] + t[1]; }
DEV float silu(float v) { return v * __builtin_amdgcn_rcpf(1.f + __expf(-v)); }
DEV float cosr(float rev) { return __builtin_amdgcn_cosf(rev); }
DEV float sinr(float rev) { return __builtin_amdgcn_sinf(rev); }
DEV f2 cis(float rev) { return f2{cosr(rev), sinr(rev)}; }
template <int N> DEV void cis_run(f2 (&v)[N], float th0, float step, float scale) {
    const float t = 2.f * cosr(step);
    v[0] = cis(th0) * scale; v[1] = cis(th0 + step) * scale;
#pragma unroll
    for (int n = 2; n < N; ++n) v[n] = t * v[n - 1] - v[n - 2];
}
DEV int col_kappa(int q, int cc) { const int pi = cc >> 1, m = pi == 0 ? 20 : 40 - pi; return (q != 0 && (cc & 1)) ? m : pi; }
DEV int col_k1(int q, int cc) { return q == 0 ? 4 * (cc & 1) : q; }
DEV int piece_off(int q, int cc, int g, int p) {
    const int u = cc >> 2, e = cc & 3;
    const int a = ((2 * q + (g >> 1)) * 20 + (g & 1) * 10 + u) * XSTR + e * 16, b = 160 * XSTR + (2 * g + e) * 16;
    const bool extra = q == 0 && u == 10 && e < 2 && g < 2;
    return (u < 10 || extra) ? WS_X + p * 64 + (u < 10 ? a : b) : OOB;
}
DEV void gemv_tile(int img, int sv, int l, f4& a0, f4& a1) {
    const int g = l >> 4, ta = img + (8 * g + ((l >> 2) & 3)) * 64 + (l & 3) * 8;
    u4 sf[5], b0[5], b1[5];
#pragma unroll
    for (int s = 0; s < 5; ++s) {
        sf[s] = lds_ld<u4>(sv + s * 64 + g * 16);
        b0[s] = lds_tr2(ta + s * 2048, ta + s * 2048 + 256); b1[s] = lds_tr2(ta + s * 2048 + 32, ta + s * 2048 + 288);
    }
    SB();
    a0 = f4{0, 0, 0, 0}; a1 = a0;
#pragma unroll
    for (int s = 0; s < 5; ++s) { a0 = mfmab(sf[s], b0[s], a0); a1 = mfmab(sf[s], b1[s], a1); }
}

#define TS(i) do { unsigned long long t_; SB(); asm volatile("s_memrealtime %0\n\ts_waitcnt lgkmcnt(0)" : "=s"(t_) :: "memory"); \
    lds_st<unsigned long long>(L_END + wave * 256 + (i) * 8, t_); SB(); } while (0)
#define TSINIT() lds_st<unsigned>(L_END + wave * 256 + l * 4, 0u)
#define TSDUMP() do { FENCE(); *(unsigned*)(ws + WS_DBG + (w * 8 + wave) * 256 + l * 4) = lds_ld<unsigned>(L_END + wave * 256 + l * 4); } while (0)

__global__ void __launch_bounds__(512, 2)
gfnet_mod_kernel(const char* a_x, const char* a_wmod, const char* a_c, char* a_ws, const char* a_wr, const char* a_wi, const char* a_br, char* a_out,
                 const char* a_gate, const char* a_up, const char* a_down) {
    __shared__ __attribute__((aligned(16))) char lds_image[L_END + STAMP * 2048];
    asm volatile("" :: "s"((LAS char*)lds_image));
    unsigned long long t_start; asm volatile("s_memrealtime %0\n\ts_waitcnt lgkmcnt(0)" : "=s"(t_start) :: "memory");
    ul4 ka, kb; ul2 kc; unsigned long long kd_;
    asm volatile("s_load_dwordx8 %0, %4, 0x0\n\ts_load_dwordx8 %1, %4, 0x20\n\ts_load_dwordx4 %2, %4, 0x40\n\ts_load_dwordx2 %3, %4, 0x50"
                 : "=&s"(ka), "=&s"(kb), "=&s"(kc), "=&s"(kd_) : "s"(__builtin_amdgcn_kernarg_segment_ptr()));
    const int w = blockIdx.x, x8 = w & 7, r = w >> 3, s2 = 32 * x8 + r;
    const int tid = threadIdx.x, wave = __builtin_amdgcn_readfirstlane(tid >> 6), l = tid & 63, g = l >> 4, c = l & 15;
    {
        unsigned long long pc; int len;
        asm volatile("s_getpc_b64 %0\n.Lgf_pc:\n\ts_mov_b32 %1, .Lgf_end-.Lgf_pc" : "=s"(pc), "=s"(len));
        dma4(mkrs((const void*)pc, r < PF_WG ? len : 0), tid * 128, L_SINK);
    }
    const bool lo = wave < 4;
    const int hidx = tid - 320;
    const bool roleB = r >= 12 || w == W_DUAL, roleF = r < 12, roleM = r < 10;
    const f4 zero4 = {0, 0, 0, 0};
    const int uid = 10 * x8 + (r < 10 ? r : r - 12), ukq = (uid >> 1) / 10, ucg = 2 * ((uid >> 1) % 10) + (uid & 1);
    const bool urgent = (wave == 4) & ((r < 10) | ((unsigned)(r - 12) < 10u));
    int ooff = 32 * c + 8 * g;
    int o2 = c < 8 ? 1024 + ooff : OOB;
    int rowb = (256 * wave + s2) * 1280;

    int uo = (ukq * 160 + (l >> 2)) * 7680 + (r < 10 ? 1280 : 0) + ucg * 64 + (l & 3) * 16;
    int cqo = tid * 8 + (wave == 4 ? ukq * 320 - 2048 : -2560);
    PIN(uo); PIN(cqo); PIN(ooff); PIN(o2);
    asm volatile("s_waitcnt lgkmcnt(0)" : "+s"(ka), "+s"(kb), "+s"(kc), "+s"(kd_), "+s"(rowb));
    const char *xin = (const char*)ka[0], *wmod = (const char*)ka[1], *cvec = (const char*)ka[2], *awr = (const char*)kb[0], *awi = (const char*)kb[1], *bwr = (const char*)kb[2];
    const char *wgate = (const char*)kc[0], *wup = (const char*)kc[1], *wdown = (const char*)kd_;
    char *ws = (char*)ka[3], *out = (char*)kb[3];
    const rsrc_t rw = mkrs(ws, WS_END), rx = mkrs(xin, 2048 * 1280), rm = mkrs(wmod, 640 * 7680), rc = mkrs(cvec, 1280);
    u2 cq;
    asm volatile("buffer_load_dwordx2 %0, %1, %2, 0 offen" : "=v"(cq) : "v"(cqo), "s"(rc));
    if (urgent) {
        PRIO(3);
        const int o = uo;
#pragma unroll
        for (int i = 0; i < 10; ++i) dma16(rm, o, i * 16 * 7680, L_UT + i * 1024);
    }
    const unsigned epoch = ld1<SC1>(rw, WS_EPOCH);
    const u2 xr0 = ld2<0>(rx, ooff, rowb), xr1 = ld2<0>(rx, 512 + ooff, rowb), xr2 = ld2<0>(rx, o2, rowb);
    TSINIT(); lds_st<unsigned long long>(L_END + wave * 256, t_start);
    TS(23);

    unsigned tag, xp0;
    f4 xc[3]; float m1;
    auto ln1 = [&]() {
        xc[0] = unb(xr0); xc[1] = unb(xr1); xc[2] = unb(xr2);
        m1 = wave_sum(hsum(xc[0] + xc[1] + xc[2])) * (1.f / 640.f);
        xc[0] -= m1; xc[1] -= m1; xc[2] -= (c < 8 ? m1 : 0.f);
        const float rs = __builtin_amdgcn_rsqf(wave_sum(hsum(xc[0] * xc[0] + xc[1] * xc[1] + xc[2] * xc[2])) * (1.f / 640.f) + 1e-5f);
        const int a = L_N + wave * NP + ooff;
        lds_st<u2>(a, pkh4(xc[0] * rs)); lds_st<u2>(a + 512, pkh4(xc[1] * rs));
        if (c < 8) lds_st<u2>(a + 1024, pkh4(xc[2] * rs));
        TS(2);
        BAR();
        TS(3);
    };
    auto settag = [&]() {
        tag = (unsigned)__builtin_amdgcn_readfirstlane((int)epoch) + 1u;
        xp0 = (tag & 1u) | ((tag & 2u) << 15);
    };
    auto xpat = [&](int e, int k) -> unsigned {
        const unsigned hb = ((tag * 0x9e3779b1u) >> (16 + 4 * e + 2 * (k & 1))), hv = (hb & 1u) | ((hb & 2u) << 15);
        return k == 0 ? xp0 : (k == 1 ? xp0 ^ LSB2 : hv);
    };

    const int bq = w == W_DUAL ? 0 : x8 >> 1, bu = w == W_DUAL ? 10 : (r - 12) % 10, bg = ((x8 & 1) << 1) | (r >= 22);
    const int seg = w == W_DUAL ? 160 : 20 * x8 + r - 12;
    u4 guf[5]; u2 wdr[16];

    auto background = [&]() {
        if (roleM) {
            if (!lo) {
                const int kq = wave - 4, o = (kq * 160 + (l >> 2)) * 7680 + 2560 + uid * 64 + (l & 3) * 16;
#pragma clang loop unroll(disable)
                for (int i = 0, j = 5 * (uid & 1); i < 10; ++i, j = j == 9 ? 0 : j + 1) dma16(rm, o, j * 16 * 7680, L_MT + kq * 10240 + j * 1024);
                DRAIN();
                f4 a0, a1; gemv_tile(L_MT + kq * 10240, L_S + kq * 320, l, a0, a1);
                if (g < 2) lds_st<float>(M_PART + l * 16 + kq * 4, g ? a1[0] : a0[0]);
            }
            BAR();
            if (wave == 4) {
                const float v0 = hsum(lds_ld<f4>(M_PART + c * 32)), v1 = hsum(lds_ld<f4>(M_PART + c * 32 + 16));
                st2<SC1>(u2{pkb(v0, v1), tag}, rw, WS_MODR + g * 10240 + uid * 128 + c * 8);
            }
        }
        if (roleF) {
            const int ft = 12 * x8 + r, kq = wave & 3, img = L_GU + wave * 5120;
            const rsrc_t rg = mkrs(lo ? wgate : wup, 640 * 3072);
            const int o = (kq * 160 + (l >> 1)) * 3072 + ft * 32 + (l & 1) * 16;
#pragma clang loop unroll(disable)
            for (int i = 0, j = r & 3; i < 5; ++i, j = j == 4 ? 0 : j + 1) dma16(rg, o, j * 32 * 3072, img + j * 1024);
            if (wave > 4) {
                const rsrc_t rd = mkrs(wdown + ft * 20480, 20480);
#pragma unroll
                for (int i = 0; i < 16; ++i) wdr[i] = ld2<0>(rd, hidx < 160 ? hidx * 8 : OOB, i * 1280);
            }
            DRAIN();
#pragma unroll
            for (int s = 0; s < 5; ++s) guf[s] = lds_tr2(img + s * 1024 + g * 256 + c * 8, img + s * 1024 + g * 256 + 128 + c * 8);
            if (wave > 4) {
#pragma unroll
                for (int i = 0; i < 16; i += 2) {
                    const u2 a = wdr[i], b = wdr[i + 1];
                    wdr[i] = u2{__builtin_amdgcn_perm(b[0], a[0], 0x05040100u), __builtin_amdgcn_perm(b[0], a[0], 0x07060302u)};
                    wdr[i + 1] = u2{__builtin_amdgcn_perm(b[1], a[1], 0x05040100u), __builtin_amdgcn_perm(b[1], a[1], 0x07060302u)};
                    PIN(wdr[i]); PIN(wdr[i + 1]);
                }
            }
        }
    };
    u4 c3[3], c3h[3];
    auto c3prep = [&]() {
#pragma unroll
        for (int T = 0; T < 3; ++T) {
            const float a40 = (float)(16 * T + c) * (1.f / 40.f), sc = (T < 2 || c < 8) ? 0.25f : 0.f;
            f2 v[6]; cis_run<6>(v, (float)(6 * g) * a40, a40, sc);
            const unsigned z = pkh(0.5f * sc, (c & 1) ? -0.5f * sc : 0.5f * sc), f = pkh(v[0][0], -v[0][1]);
            c3[T] = u4{g == 0 ? z : f, pkh(v[1][0], -v[1][1]), pkh(v[2][0], -v[2][1]), pkh(v[3][0], -v[3][1])};
            c3h[T] = u4{pkh(v[4][0], -v[4][1]), pkh(v[5][0], -v[5][1]), 0, 0};
            PIN(c3[T]); PIN(c3h[T]);
        }
    };
    u2 wrr[3];
    auto wrload = [&]() { const rsrc_t rb = mkrs(bwr, 1280); wrr[0] = ld2<0>(rb, ooff); wrr[1] = ld2<0>(rb, 512 + ooff); wrr[2] = ld2<0>(rb, o2); };
    f4 vga[3], vc1[3], vsb[3], vg1[3], vg0[3], vu[3]; float usum, rconst;
    auto modprep = [&]() {
        f4 a = zero4, b = zero4;
#pragma unroll
        for (int T = 0; T < 3; ++T) {
            const int o = L_MOD + T * 512 + ooff;
            const f4 wr = unb(wrr[T]), gb = unb(lds_ld<u2>(o + 3840));
            vga[T] = unb(lds_ld<u2>(o)) * 0.2f; vsb[T] = unb(lds_ld<u2>(o + 1280)); vc1[T] = unb(lds_ld<u2>(o + 2560)) + 1.f;
            vg1[T] = gb * vc1[T]; vg0[T] = gb * vsb[T] + m1; vu[T] = vc1[T] * wr;
            a += vu[T]; b += vsb[T] * wr;
        }
        const f2 a2 = lo2(a) + hi2(a), b2v = lo2(b) + hi2(b);
        const unsigned t = as_u(row_sum(fold4(a2[0], a2[1], b2v[0], b2v[1])));
        usum = as_f(rdl(t, 0)) + as_f(rdl(t, 16)); rconst = as_f(rdl(t, 32)) + as_f(rdl(t, 48));
#pragma unroll
        for (int T = 0; T < 3; ++T) { PIN(vga[T]); PIN(vsb[T]); PIN(vc1[T]); PIN(vg1[T]); PIN(vg0[T]); PIN(vu[T]); }
        PIN(usum); PIN(rconst);
    };

    if (lo) {
        u4 a1r[3][2], a1i[3][2], a1h[3], fre, fim; f4 atr[3], ati[3]; int xo[3];
        lds_st<u4>(L_CA + tid * 16, u4{SENT, SENT, SENT, SENT});
        if (tid < 32) lds_st<u4>(L_CA + 4096 + tid * 16, u4{SENT, SENT, SENT, SENT});
#pragma unroll
        for (int T = 0; T < 3; ++T) {
            const int cc = 16 * T + c;
            const float kap = (float)col_kappa(wave, cc);
            f2 v[10]; cis_run<10>(v, (float)g * kap * 0.25f, kap * (1.f / 40.f), 1.f);
#pragma unroll
            for (int i = 0; i < 10; ++i) {
                const unsigned re = pkh(v[i][0], v[i][1]), im = pkh(-v[i][1], v[i][0]);
                if (i < 8) { a1r[T][i >> 2][i & 3] = re; a1i[T][i >> 2][i & 3] = im; } else { a1h[T][i - 8] = re; a1h[T][i - 6] = im; }
            }
            const float ph = (float)(s2 * col_k1(wave, cc)) * (1.f / 2048.f), k640 = kap * (1.f / 640.f);
#pragma unroll
            for (int i = 0; i < 4; ++i) { const float th = -((float)(4 * g + i) * k640 + ph); atr[T][i] = cosr(th); ati[T][i] = sinr(th); }
            xo[T] = piece_off(wave, cc, g, s2);
        }
        if (wave == 0) {
            const unsigned sel = (c & 1) ? 0x01000c0cu : 0x0c0c0100u;
#pragma unroll
            for (int T = 0; T < 3; ++T)
#pragma unroll
                for (int i = 0; i < 4; ++i) {
                    a1r[T][0][i] = __builtin_amdgcn_perm(0u, a1r[T][0][i], sel); a1r[T][1][i] = __builtin_amdgcn_perm(0u, a1r[T][1][i], sel);
                    a1i[T][0][i] = __builtin_amdgcn_perm(0u, a1i[T][0][i], sel); a1i[T][1][i] = __builtin_amdgcn_perm(0u, a1i[T][1][i], sel);
                    a1h[T][i] = __builtin_amdgcn_perm(0u, a1h[T][i], sel);
                }
        }
#pragma unroll
        for (int i = 0; i < 4; ++i) { const f2 v = cis((float)((4 * g + i) * c) * (1.f / 16.f)) * 0.25f; fre[i] = pkh(v[0], v[1]); fim[i] = pkh(-v[1], v[0]); }
#pragma unroll
        for (int T = 0; T < 3; ++T) { PIN(a1r[T][0]); PIN(a1r[T][1]); PIN(a1i[T][0]); PIN(a1i[T][1]); PIN(a1h[T]); PIN(atr[T]); PIN(ati[T]); PIN(xo[T]); }
        PIN(fre); PIN(fim);
        constexpr unsigned P = 0x3800, M = 0xb800, p = 0x35a8, m = 0xb5a8;
        const bool odd = wave & 1; const unsigned pm = wave == 1 ? 0u : 0x80000000u;
        const u4 gr = odd ? u4{(P | p << 16) ^ pm, (m << 16) ^ pm, (M | m << 16) ^ pm, (p << 16) ^ pm} : (wave ? u4{P, M, P, M} : u4{P | P << 16, P | P << 16, P | P << 16, P | P << 16});
        const u4 gi = odd ? u4{m << 16, (M | m << 16) ^ (pm >> 16), p << 16, (P | p << 16) ^ (pm >> 16)} : (wave ? u4{M << 16, P << 16, M << 16, P << 16} : u4{P | M << 16, P | M << 16, P | M << 16, P | M << 16});
        TS(1);
        settag();
        u4 xp = {xpat(c & 3, 0), xpat(c & 3, 1), xpat(c & 3, 2), xpat(c & 3, 3)};
        u4 xpx = xp;
        if (c >= 8) { xpx[2] = xpat(2 * g + (c & 1), 2); xpx[3] = xpat(2 * g + (c & 1), 3); }
        int go = tid < 160 ? WS_CA + x8 * 10240 + tid * 16 : OOB;
        int cw = L_CA + (tid & 7) * 2 * CAP + ((tid >> 3) / 5) * 32 + ((tid >> 3) % 5) * 4, cr = L_CA + c * CAP + g * 32;
        PIN(xp); PIN(xpx); PIN(go); PIN(cw); PIN(cr);
        ln1();
        asm volatile("" :: "v"(cq));
        PRIO(2);
        nap<D_CA>();
        u4 gv[4];
        if (wave < 3) {
#pragma unroll
            for (int k = 0; k < 4; ++k) gv[k] = ld4<SC1>(rw, go, k * 2560);
        }
        unsigned y[10];
        {
            const int na = L_N + ((l >> 2) & 3) * NP + (l & 3) * 8 + 320 * g;
            u4 v[10];
#pragma unroll
            for (int i = 0; i < 10; ++i) v[i] = lds_tr2(na + 32 * i, na + 32 * i + 4 * NP);
            SB();
#pragma unroll
            for (int i = 0; i < 10; ++i) {
                float re = 0.f, im = 0.f;
#pragma unroll
                for (int k = 0; k < 4; ++k) { re = __builtin_amdgcn_fdot2(as_h2(v[i][k]), as_h2(gr[k]), re, false); im = __builtin_amdgcn_fdot2(as_h2(v[i][k]), as_h2(gi[k]), im, false); }
                y[i] = pkh(re, im);
            }
#pragma unroll
            for (int i = 0; i < 10; ++i) PIN(y[i]);
        }
        TS(4);
        unsigned addc = 0x30003000u;
#pragma clang loop unroll(disable)
        for (int pass = 0;; ++pass) {
            if (wave < 3) {
                for (int n = 0; n < SPIN; ++n) {
                    unsigned d = 0;
#pragma unroll
                    for (int k = 0; k < 4; ++k) d |= (gv[k][1] ^ tag) | (gv[k][3] ^ tag);
                    const bool bad = d != 0 && tid < 160;
                    if (!any(bad)) break;
                    FENCE();
                    if (bad) {
#pragma unroll
                        for (int k = 0; k < 4; ++k) gv[k] = ld4<SC1>(rw, go, k * 2560);
                    }
                }
                const h2 e8 = as_h2(0x30003000u);
                const h2 s0 = ((as_h2(gv[0][0]) + as_h2(gv[1][0])) + (as_h2(gv[2][0]) + as_h2(gv[3][0]))) * e8 + as_h2(addc);
                const h2 s1 = ((as_h2(gv[0][2]) + as_h2(gv[1][2])) + (as_h2(gv[2][2]) + as_h2(gv[3][2]))) * e8 + as_h2(addc);
                if (tid < 160) { lds_st<unsigned>(cw, as_u(s0)); lds_st<unsigned>(cw + CAP, as_u(s1)); }
            }
            if (pass == 0 || wave == 0) {
                u4 ca; unsigned cb;
                for (int n = 0; n < (SPIN << 4); ++n) {
                    FENCE();
                    ca = lds_ld<u4>(cr); cb = lds_ld<unsigned>(cr + 16);
                    if (!any(max(max(max(ca[0], ca[1]), ca[2]), max(ca[3], cb)) == SENT)) break;
                }
                TS(5);
                const unsigned cv[5] = {ca[0], ca[1], ca[2], ca[3], cb};
                unsigned z[10];
#pragma unroll
                for (int i = 0; i < 10; ++i) { const h2 t = as_h2(cv[i >> 1]); const _Float16 s = t[i & 1]; z[i] = as_u(as_h2(y[i]) * h2{s, s}); }
                const u4 d0 = {z[0], z[1], z[2], z[3]}, d1 = {z[4], z[5], z[6], z[7]}, d2r = {z[8], z[9], 0, 0}, d2i = {0, 0, z[8], z[9]};
                f4 re[3], im[3]; u4 uu[3];
#pragma unroll
                for (int T = 0; T < 3; ++T) {
                    re[T] = mfma(d2r, a1h[T], mfma(d1, a1r[T][1], mfma(d0, a1r[T][0], zero4)));
                    im[T] = mfma(d2i, a1h[T], mfma(d1, a1i[T][1], mfma(d0, a1i[T][0], zero4)));
                }
#pragma unroll
                for (int T = 0; T < 3; ++T) uu[T] = cmul_pack(re[T], im[T], atr[T], ati[T]);
#pragma unroll
                for (int T = 0; T < 3; ++T) { re[T] = mfma(fre, uu[T], zero4); im[T] = mfma(fim, uu[T], zero4); }
#pragma unroll
                for (int T = 0; T < 3; ++T) st4<SC1>((pack4(re[T], im[T]) & ~LSB2) | (T == 2 ? xpx : xp), rw, xo[T]);
            }
            if (w != W_SA || pass == 1) break;
            go = tid < 160 ? WS_SA + tid * 16 : OOB; cw += 2304; cr += 2304; addc = 0;
#pragma unroll
            for (int k = 0; k < 4; ++k) gv[k] = ld4<SC1>(rw, go, k * 2560);
#pragma unroll
            for (int i = 0; i < 10; ++i) y[i] = 0x3c00u;
#pragma unroll
            for (int T = 0; T < 3; ++T) xo[T] = piece_off(0, 16 * T + c, g, 256);
        }
        TS(6);
        PRIO(0);
        if (!roleB) BAR();
        f4 ctr[3], cti[3]; u4 c2r, c2i, c2r0, c2i0; int co[3];
#pragma unroll
        for (int i = 0; i < 3; ++i) {
#pragma unroll
            for (int e = 0; e < 4; ++e) {
                const int cc = 4 * (3 * wave + i) + e;
                const float th = (float)(c * col_kappa(g, cc)) * (1.f / 640.f) + (float)(s2 * col_k1(g, cc)) * (1.f / 2048.f);
                ctr[i][e] = cosr(th); cti[i][e] = sinr(th);
            }
            co[i] = piece_off(c >> 2, 4 * (3 * wave + i) + (c & 3), g, s2);
        }
        {
            const int s1 = c >> 1, h = c & 1;
            const float th = (float)(s1 * g) * 0.125f, cs = 0.5f * cosr(th), si = 0.5f * sinr(th), sg = (s1 & 1) ? -0.5f : 0.5f;
            const unsigned r0 = pkh(cs, -si), i0 = pkh(si, cs), r1 = g ? r0 : pkh(sg, 0.f), i1 = g ? pkh(-si, -cs) : pkh(0.f, sg);
            c2r = h ? u4{0, 0, r0, r1} : u4{r0, r1, 0, 0}; c2i = h ? u4{0, 0, i0, i1} : u4{i0, i1, 0, 0};
            const unsigned d2 = pkh(2.f * cs, -2.f * si);
            c2r0 = c2r; c2i0 = c2i;
            if (wave == 0 && !h) { c2r0[0] = g ? d2 : r0; c2r0[1] = g ? 0u : r1; c2i0[0] = 0u; c2i0[1] = g ? d2 : 0u; }
        }
        u4 gre = fre ^ 0x80000000u, gim = fim ^ 0x00008000u;
        const unsigned xq = xp0 ^ 0x10000u;
        unsigned m1v = co[1] == OOB ? 0u : LSB2, m2v = co[2] == OOB ? 0u : LSB2;
        int e3w = L_E3 + (32 * g + c) * E3P + wave * 32;
#pragma unroll
        for (int i = 0; i < 3; ++i) { PIN(ctr[i]); PIN(cti[i]); PIN(co[i]); }
        PIN(c2r); PIN(c2i); PIN(c2r0); PIN(c2i0); PIN(gre); PIN(gim); PIN(m1v); PIN(m2v); PIN(e3w);
        TS(7);
        if (roleB) {
            int so = WS_X + seg * XSTR + (64 * g + 4 * wave) * 64 + c * 16;
            int ea = L_E1 + c * E1P + (16 * (g >> 1) + 4 * wave + 2 * (g & 1)) * 32, eb = L_E2 + wave * E2P + l * 8;
            PIN(so); PIN(ea); PIN(eb);
            nap<D_X1>();
            PRIO(2);
            u4 v[4];
            TS(8);
#pragma unroll
            for (int i = 0; i < 4; ++i) v[i] = ld4<SC1>(rw, so + i * 1024);
            PRIO(0);
            c3prep();
            PRIO(2);
            for (int n = 0; n < SPIN; ++n) {
                const u4 d = ((v[0] ^ xp) | (v[1] ^ xp)) | ((v[2] ^ xp) | (v[3] ^ xp));
                unsigned b = ((d[0] | d[1]) | (d[2] | d[3])) & LSB2;
                if (!any(b != 0)) break;
                b |= dpp<0xb1>(b); b |= dpp<0x4e>(b);
                FENCE();
                if (b) {
#pragma unroll
                    for (int i = 0; i < 4; ++i) v[i] = ld4<SC1>(rw, so + i * 1024);
                }
            }
            TS(9);
            f4 re[4], im[4];
#pragma unroll
            for (int mu = 0; mu < 4; ++mu) {
                const u4 d = {v[0][mu], v[1][mu], v[2][mu], v[3][mu]};
                re[mu] = mfma(d, fre, zero4); im[mu] = mfma(d, fim, zero4);
            }
#pragma unroll
            for (int h = 0; h < 2; ++h) {
                const u2 a = pkh4(re[2 * h]), b = pkh4(re[2 * h + 1]), cc = pkh4(im[2 * h]), d = pkh4(im[2 * h + 1]);
                lds_st<u4>(ea + 16 * h, u4{a[0], a[1], b[0], b[1]}); lds_st<u4>(ea + 32 + 16 * h, u4{cc[0], cc[1], d[0], d[1]});
            }
            BAR();
            BAR();
            u4 d[4];
#pragma unroll
            for (int mu = 0; mu < 4; ++mu) d[mu] = lds_tr2(eb + mu * 1024, eb + mu * 1024 + 512);
            SB();
#pragma unroll
            for (int mu = 0; mu < 4; ++mu) { re[mu] = mfma(gre, d[mu], zero4); im[mu] = mfma(gim, d[mu], zero4); }
#pragma unroll
            for (int i = 0; i < 4; ++i)
                st4<SC1>(u4{(pkh(re[0][i], im[0][i]) & ~LSB2) | xq, pkh(re[1][i], im[1][i]), pkh(re[2][i], im[2][i]), pkh(re[3][i], im[3][i])}, rw, so + i * 1024);
            PRIO(0);
            TS(10);
        } else c3prep();
        wrload();
        background();
        BAR();
        TS(11);
        if (roleB) nap<D_X2>();
        else {
            for (int n = 0; n < SPIN; ++n) { FENCE(); if (!any(((ld1<SC1>(rw, WS_X + (20 * x8 + r) * XSTR + s2 * 64) ^ xq) & LSB2) != 0)) break; }
            nap<D_CAN>();
        }
        u4 v[3];
        TS(12);
#pragma unroll
        for (int i = 0; i < 3; ++i) v[i] = ld4<SC1>(rw, co[i]);
        modprep();
        PRIO(2);
        for (int n = 0; n < SPIN; ++n) {
            const bool bad = (((v[0][0] ^ xq) & LSB2) | ((v[1][0] ^ xq) & m1v) | ((v[2][0] ^ xq) & m2v)) != 0;
            if (!any(bad)) break;
            FENCE();
            if (bad) {
#pragma unroll
                for (int i = 0; i < 3; ++i) v[i] = ld4<SC1>(rw, co[i]);
            }
        }
        TS(13);
        f4 re[3], im[3]; u4 t[3];
#pragma unroll
        for (int i = 0; i < 3; ++i) { re[i] = mfma(v[i], gre, zero4); im[i] = mfma(v[i], gim, zero4); }
#pragma unroll
        for (int i = 0; i < 3; ++i) t[i] = cmul_pack(re[i], im[i], ctr[i], cti[i]);
#pragma unroll
        for (int i = 0; i < 3; ++i) { re[i] = mfma(i ? c2r : c2r0, t[i], zero4); im[i] = mfma(i ? c2i : c2i0, t[i], zero4); }
#pragma unroll
        for (int i = 0; i < 3; ++i) t[i] = pack4(re[i], im[i]);
        lds_st<u4>(e3w, u4{t[0][0], t[0][1], t[1][0], t[1][1]}); lds_st<u4>(e3w + 16 * E3P, u4{t[0][2], t[0][3], t[1][2], t[1][3]});
        lds_st<u2>(e3w + 16, u2{t[2][0], t[2][1]}); lds_st<u2>(e3w + 16 * E3P + 16, u2{t[2][2], t[2][3]});
        PRIO(0);
    } else {
        if (urgent) asm volatile("s_waitcnt vmcnt(14)" ::: "memory"); else asm volatile("s_waitcnt vmcnt(4)" ::: "memory");
        u2 cq2; asm volatile("v_mov_b64 %0, %1" : "=v"(cq2) : "v"(cq));
        const f4 cv = unb(cq2);
        lds_st<u2>(wave == 4 ? L_SU + l * 8 : L_S + hidx * 8, u2{pkb(silu(cv[0]), silu(cv[1])), pkb(silu(cv[2]), silu(cv[3]))});
        if (wave > 4) {
            const int q = x8 >> 1, ri = hidx / 6, ln = hidx % 6, cls = q == 0 ? (ri >> 3) * 4 : ((x8 & 1) ? 8 - q : q);
            const int o = ri < (q == 0 ? 16 : 8) ? ((cls + 8 * (8 * r + (ri & 7))) * 642 + ln * 128) & ~3 : OOB;
            if (wave < 7) { dma4(mkrs(awr, 2048 * 642), o, L_SINK); dma4(mkrs(awi, 2048 * 642), o, L_SINK); }
        }
        settag();
        if (urgent) {
            asm volatile("s_waitcnt vmcnt(3)" ::: "memory");
            TS(24);
            f4 a0, a1; gemv_tile(L_UT, L_SU, l, a0, a1);
            const u2 gr = {pkh(a0[0], a1[0]), tag};
            const int o = (r < 10 ? WS_CA : WS_SA) + g * 10240 + (ukq * 20 + ucg) * 128 + c * 8;
            st2<SC1>(gr, rw, o); st2<SC1>(gr, rw, o, 40960);
            TS(1);
        }
        PRIO(1);
        ln1();
        PRIO(0);
        if (roleB) {
            const int wq = wave - 4, e = c & 3, mulo = c >> 2, cc = 4 * bu + e;
            const int k1 = col_k1(bq, cc), kd = bu == 10 ? 20 + 40 * (4 * (e >> 1) + mulo) : col_kappa(bq, cc) + 40 * (4 * bg + mulo);
            const bool edge = kd == 0 || kd == 320;
            const int col = bg < 2 ? kd : 640 - kd, ks0 = k1 + 8 * wq + 512 * g;
            const rsrc_t rr = mkrs(awr, 2048 * 642), ri = mkrs(awi, 2048 * 642);
            f4 wr[4], wi[4];
#pragma unroll
            for (int j = 0; j < 4; ++j) { wr[j] = zero4; wi[j] = zero4; }
            const int np = (bu == 0 && !(bg & 1)) ? 2 : 1;
            nap<D_W>();
            TS(5);
#pragma clang loop unroll(disable)
            for (int ps = 0; ps < np; ++ps) {
                const bool mir = (bg >= 2) != (ps == 1), off = ps && !edge;
                const float sr = edge ? 0.5f : 1.f, si = mir ? -sr : sr;
                const int vo = off ? OOB : ((mir ? 1568 - ks0 : ks0) * 321 + col) * 2, v0 = off ? OOB : (((mir ? -ks0 : ks0) & 2047) * 321 + col) * 2;
                unsigned short tr[16], ti[16];
#pragma unroll
                for (int n = 0; n < 16; ++n) {
                    const int so = n ? 32 * 642 * (mir ? 15 - n : n) : 0;
                    tr[n] = __builtin_amdgcn_raw_buffer_load_b16(rr, n ? vo : v0, so, 0); ti[n] = __builtin_amdgcn_raw_buffer_load_b16(ri, n ? vo : v0, so, 0);
                }
                SB();
#pragma unroll
                for (int n = 0; n < 16; ++n) { wr[n & 3][n >> 2] += sr * bflo(tr[n]); wi[n & 3][n >> 2] += si * bflo(ti[n]); }
            }
            TS(6);
            u4 b2r[4], b2i[4], b3r[4], b3i[4];
            f2 v3[16]; cis_run<16>(v3, (float)(c * (wq + 64 * g)) * (1.f / 256.f), (float)c * (1.f / 64.f), 0.25f);
#pragma unroll
            for (int j = 0; j < 4; ++j)
#pragma unroll
                for (int i = 0; i < 4; ++i) {
                    const f2 v2 = cis((float)(4 * g + i) * ((float)(wq + 4 * j + 16 * c) * (1.f / 256.f)));
                    b2r[j][i] = pkh(v2[0], v2[1]); b2i[j][i] = pkh(-v2[1], v2[0]);
                    b3r[j][i] = pkh(v3[j + 4 * i][0], -v3[j + 4 * i][1]); b3i[j][i] = pkh(v3[j + 4 * i][1], v3[j + 4 * i][0]);
                }
            int ra = L_E1 + wq * E1P + l * 8, wa = L_E2 + (c >> 2) * E2P + g * 1024 + (16 * (wq >> 1) + 2 * (wq & 1)) * 32 + (c & 3) * 8;
#pragma unroll
            for (int j = 0; j < 4; ++j) { PIN(b2r[j]); PIN(b2i[j]); PIN(b3r[j]); PIN(b3i[j]); PIN(wr[j]); PIN(wi[j]); }
            f4 sar = zero4, sai = zero4;
            if (bq == 0 && wave == 4) {
                const unsigned pt = xpat(e, mulo);
                unsigned t;
                for (int n = 0; n < SPIN; ++n) { FENCE(); t = ld1<SC1>(rw, WS_X + seg * XSTR + 256 * 64 + e * 16 + mulo * 4); if (!any(((t ^ pt) & LSB2) != 0)) break; }
                const h2 th = as_h2(t);
                if (g == 0 && k1 == 0) { sar[0] = 256.f * (float)th[0]; sai[0] = 256.f * (float)th[1]; }
            }
            PIN(sar); PIN(sai); PIN(ra); PIN(wa);
            PRIO(2);
            TS(7);
            BAR();
            TS(8);
            u4 d[4]; f4 re[4], im[4];
#pragma unroll
            for (int j = 0; j < 4; ++j) d[j] = lds_tr2(ra + 4 * j * E1P, ra + 4 * j * E1P + 512);
            SB();
#pragma unroll
            for (int j = 0; j < 4; ++j) { re[j] = mfma(b2r[j], d[j], j ? zero4 : sar); im[j] = mfma(b2i[j], d[j], j ? zero4 : sai); }
#pragma unroll
            for (int j = 0; j < 4; ++j) d[j] = cmul_pack(re[j], im[j], wr[j], wi[j]);
#pragma unroll
            for (int j = 0; j < 4; ++j) { re[j] = mfma(d[j], b3r[j], zero4); im[j] = mfma(d[j], b3i[j], zero4); }
#pragma unroll
            for (int j = 0; j < 4; ++j) { lds_st<u2>(wa + 128 * j, pkh4(re[j])); lds_st<u2>(wa + 128 * j + 32, pkh4(im[j])); }
            BAR();
            PRIO(0);
            TS(9);
            if (w == 255 && wave == 4) st1<SC1>(tag, rw, l == 0 ? WS_EPOCH : OOB);
        } else BAR();
        wrload();
        background();
        {
            const int i0 = tid - 256, o = WS_MODR + (x8 & 3) * 10240 + i0 * 16;
            u4 v[3];
            for (int n = 0; n < SPIN; ++n) {
                FENCE();
                v[0] = ld4<SC1>(rw, o); v[1] = ld4<SC1>(rw, o, 4096); v[2] = ld4<SC1>(rw, wave < 6 ? o : o - 2048, 8192);
                unsigned d = 0;
#pragma unroll
                for (int k = 0; k < 3; ++k) d |= (v[k][1] ^ tag) | (v[k][3] ^ tag);
                if (!any(d != 0)) break;
            }
#pragma unroll
            for (int k = 0; k < 3; ++k) lds_st<u2>(L_MOD + k * 2048 + i0 * 8, u2{v[k][0], v[k][2]});
        }
        TS(10);
        BAR();
        c3prep();
        modprep();
        TS(11);
    }
    int e3r = L_E3 + (16 * wave + c) * E3P + g * 32, ko = l < 8 ? WS_KEY + l * 4096 + w * 16 : OOB;
    int sw_ = M_STAT + wave * 16 + g * 4, rid = 2047 - (256 * (l & 7) + s2);
    PIN(e3r); PIN(ko); PIN(sw_); PIN(rid);
    BAR();
    TS(14);

    {
        const u4 d = lds_ld<u4>(e3r); const u2 dh = lds_ld<u2>(e3r + 16);
        SB();
        f4 y[3];
#pragma unroll
        for (int T = 0; T < 3; ++T) y[T] = mfma(u4{dh[0], dh[1], 0, 0}, c3h[T], mfma(d, c3[T], zero4));
#pragma unroll
        for (int T = 0; T < 3; ++T) xc[T] += vga[T] * y[T];
        const f4 t = xc[0] + xc[1] + xc[2];
        f2 q = lo2(xc[0]) * lo2(xc[0]), u = lo2(xc[0]) * lo2(vu[0]);
        q += hi2(xc[0]) * hi2(xc[0]); u += hi2(xc[0]) * hi2(vu[0]);
#pragma unroll
        for (int T = 1; T < 3; ++T) { q += lo2(xc[T]) * lo2(xc[T]); u += lo2(xc[T]) * lo2(vu[T]); q += hi2(xc[T]) * hi2(xc[T]); u += hi2(xc[T]) * hi2(vu[T]); }
        lds_st<float>(sw_, row_sum(fold4(hsum(t), q[0] + q[1], u[0], u[1])));
    }
    BAR();
    const f4 st = lds_ld<f4>(M_STAT + (l & 7) * 16);
    const float m2l = st[0] * (1.f / 640.f);
    const float rsl = __builtin_amdgcn_rsqf(fmaxf(st[1] * (1.f / 640.f) - m2l * m2l, 0.f) + 1e-5f);
    const unsigned sbits = as_u(rsl * ((st[2] + st[3]) - m2l * usum) + rconst);
    const unsigned keyl = ((sbits ^ ((unsigned)((int)sbits >> 31) | 0x80000000u)) & 0xfffff800u) | (unsigned)rid;
    unsigned kmax = max(keyl, dpp<0x124>(keyl)); kmax = max(kmax, dpp<0x122>(kmax)); kmax = max(kmax, dpp<0x121>(kmax));
    if (wave == 0) st2<SC1>(u2{kmax, tag}, rw, ko);
    TS(15);
    const unsigned key = rdl(kmax, 0);
    const bool cand = (int)((2047 - (key & 2047)) >> 8) == wave;
    const float m2 = as_f(rdl(as_u(m2l), wave)), rs2 = as_f(rdl(as_u(rsl), wave));
    const rsrc_t ro = mkrs(out, 2048 * 1280);
    u2 ov[3]; f4 z[3];
#pragma unroll
    for (int T = 0; T < 3; ++T) z[T] = xc[T] * rs2 - m2 * rs2;
    if (cand) {
        PRIO(2);
        const u2 h0 = pkb4(z[0] * vc1[0] + vsb[0]), h1 = pkb4(z[1] * vc1[1] + vsb[1]), h2v = pkb4(z[2] * vc1[2] + vsb[2]);
        st4<SC1>(u4{h0[0], h0[1], h1[0], tag}, rw, WS_ROW + w * 2048 + l * 16);
        st4<SC1>(u4{h1[1], h2v[0], h2v[1], tag}, rw, WS_ROW + w * 2048 + 1024 + l * 16);
    }
#pragma unroll
    for (int T = 0; T < 3; ++T) ov[T] = pkb4(xc[T] + (z[T] * vg1[T] + vg0[T]));
    bool wr_out = true;
    if (cand) {
        PIN(ov[0]); PIN(ov[1]); PIN(ov[2]);
        if (roleF) nap<D_KEY>(); else nap<D_KEYB>();
        const int o = WS_KEY + x8 * 4096 + l * 16;
        u2 k[4];
        TS(16);
        for (int n = 0; n < SPIN; ++n) {
            FENCE();
#pragma unroll
            for (int i = 0; i < 4; ++i) k[i] = ld2<SC1>(rw, o + i * 1024);
            if (!any((((k[0][1] ^ tag) | (k[1][1] ^ tag)) | ((k[2][1] ^ tag) | (k[3][1] ^ tag))) != 0)) break;
        }
        const unsigned win = wave_umax(max(max(k[0][0], k[1][0]), max(k[2][0], k[3][0])));
        TS(17);
        wr_out = win != key;
        if (roleF) {
            const int sw = (2047 - (win & 2047)) & 255, ro2 = WS_ROW + ((sw & 31) * 8 + (sw >> 5)) * 2048;
            u4 v0, v1;
            for (int n = 0; n < SPIN; ++n) {
                FENCE();
                v0 = ld4<SC1>(rw, l * 16, ro2); v1 = ld4<SC1>(rw, l * 16 + 1024, ro2);
                if (!any(((v0[3] ^ tag) | (v1[3] ^ tag)) != 0)) break;
            }
            lds_st<u2>(L_H + ooff, u2{v0[0], v0[1]}); lds_st<u2>(L_H + 512 + ooff, u2{v0[2], v1[0]}); lds_st<u2>(L_H + 1024 + ooff, u2{v1[1], v1[2]});
            lds_st<unsigned>(M_WIN, win);
            TS(18);
        }
    }
    if (!roleF || !cand) {
        if (wr_out) { st2<0>(ov[0], ro, ooff, rowb); st2<0>(ov[1], ro, 512 + ooff, rowb); st2<0>(ov[2], ro, o2, rowb); }
        else {
#pragma unroll
            for (int T = 0; T < 3; ++T) st4<SC1>(u4{ov[T][0], tag, ov[T][1], tag}, rw, WS_BASE + T * 1024 + 2 * ooff);
        }
    }
    if (!roleF) { TSDUMP(); return; }

    {
        int ha = L_H + (wave & 3) * 320 + g * 16, pa = M_PART + (wave >> 2) * 256 + c * 16 + (wave & 3) * 4;
        PIN(ha); PIN(pa);
        BAR();
        u4 hf[5];
#pragma unroll
        for (int s = 0; s < 5; ++s) hf[s] = lds_ld<u4>(ha + s * 64);
        SB();
        f4 a = zero4;
#pragma unroll
        for (int s = 0; s < 5; ++s) a = mfmab(hf[s], guf[s], a);
        lds_st<float>(pa, a[0]);
    }
    BAR();
    TS(19);
    const unsigned ytag = (tag >> (8 * (l & 3))) & 0xffu;
    if (wave > 4) {
        const float act = silu(hsum(lds_ld<f4>(M_PART + c * 16))) * hsum(lds_ld<f4>(M_PART + 256 + c * 16));
        const unsigned pr = pkb(act, dppf<0x101>(act));
        f4 a = zero4;
#pragma unroll
        for (int i = 0; i < 8; ++i) {
            const b2 s = as_b2(rdl(pr, 2 * i));
            const u2 p0 = wdr[2 * i], p1 = wdr[2 * i + 1];
            a[0] = __builtin_amdgcn_fdot2_f32_bf16(as_b2(p0[0]), s, a[0], false); a[1] = __builtin_amdgcn_fdot2_f32_bf16(as_b2(p0[1]), s, a[1], false);
            a[2] = __builtin_amdgcn_fdot2_f32_bf16(as_b2(p1[0]), s, a[2], false); a[3] = __builtin_amdgcn_fdot2_f32_bf16(as_b2(p1[1]), s, a[3], false);
        }
        st4<SC1>(u4{(as_u(a[0]) & ~0xffu) | ytag, as_u(a[1]), as_u(a[2]), as_u(a[3])}, rw, hidx < 160 ? WS_YP + (12 * x8 + r) * 2560 + hidx * 16 : OOB);
        TS(20);
    }
    if (cand) {
        if (wr_out) { st2<0>(ov[0], ro, ooff, rowb); st2<0>(ov[1], ro, 512 + ooff, rowb); st2<0>(ov[2], ro, o2, rowb); }
        else {
#pragma unroll
            for (int T = 0; T < 3; ++T) st4<SC1>(u4{ov[T][0], tag, ov[T][1], tag}, rw, WS_BASE + T * 1024 + 2 * ooff);
        }
    }
    if (wave == 4 && r < 5) {
        const unsigned win = (unsigned)__builtin_amdgcn_readfirstlane((int)lds_ld<unsigned>(M_WIN));
        const int row = 2047 - (win & 2047), col = 16 * (5 * x8 + r) + 4 * (l & 3) + g;
        const unsigned kb = (win & 0xfffff800u) | 0x400u;
        const float sc = as_f(kb ^ ((kb >> 31) ? 0x80000000u : 0xffffffffu));
        float gs = bflo(lds_ld<unsigned short>(L_MOD + 3840 + col * 2)) * __builtin_amdgcn_rcpf(1.f + __expf(-sc));
        int yo = WS_YP + (l >> 2) * 2560 + (5 * x8 + r) * 64 + (l & 3) * 16, bo = WS_BASE + (col >> 1) * 8, oo = c < 4 ? row * 1280 + col * 2 : OOB;
        PIN(gs); PIN(yo); PIN(bo); PIN(oo);
        nap<D_YP>();
        u4 v[6]; u2 bs;
        TS(20);
        for (int n = 0; n < SPIN; ++n) {
            FENCE();
#pragma unroll
            for (int i = 0; i < 6; ++i) v[i] = ld4<SC1>(rw, yo, i * 40960);
            bs = ld2<SC1>(rw, bo);
            unsigned d = 0;
#pragma unroll
            for (int i = 0; i < 6; ++i) d |= v[i][0] ^ ytag;
            if (!any(((d & 0xffu) | (bs[1] ^ tag)) != 0)) break;
        }
        TS(21);
        const f4 a = ((as_f4(v[0]) + as_f4(v[1])) + (as_f4(v[2]) + as_f4(v[3]))) + (as_f4(v[4]) + as_f4(v[5]));
        float t = fold4(a[0], a[1], a[2], a[3]);
        t += dppf<0x124>(t); t += dppf<0x128>(t);
        const float base = (g & 1) ? bfhi(bs[0]) : bflo(bs[0]);
        __builtin_amdgcn_raw_buffer_store_b16((unsigned short)pkb(base + gs * t, 0.f), ro, oo, 0, 0);
        TS(22);
        asm volatile(".Lgf_end:");
    }
    TSDUMP();
}

__global__ void __launch_bounds__(1024) k_amp(const char* ws) {
    typedef unsigned long long u64;
    __shared__ u64 r0[1024], r1[1024], r2[1024]; __shared__ unsigned cn[1024];
    const u64* t = (const u64*)(ws + WS_DBG);
    u64 mn = ~0ull, mx = 0, sm = 0; unsigned n = 0;
    for (int i = threadIdx.x; i < 2048; i += 1024) {
        const u64 a = t[i * 32]; mn = a < mn ? a : mn;
        if ((AMP_MASK >> (i & 7)) & 1) for (int k = (AMP_IDX < 0 ? 0 : AMP_IDX); k <= (AMP_IDX < 0 ? 31 : AMP_IDX); ++k) { const u64 b = t[i * 32 + k]; if (b) { mx = b > mx ? b : mx; sm += b & 0xffffffffffull; ++n; } }
    }
    r0[threadIdx.x] = mn; r1[threadIdx.x] = mx; r2[threadIdx.x] = sm; cn[threadIdx.x] = n;
    __syncthreads();
    for (int o = 512; o; o >>= 1) { if (threadIdx.x < o) { const int j = threadIdx.x + o; if (r0[j] < r0[threadIdx.x]) r0[threadIdx.x] = r0[j]; if (r1[j] > r1[threadIdx.x]) r1[threadIdx.x] = r1[j]; r2[threadIdx.x] += r2[j]; cn[threadIdx.x] += cn[j]; } __syncthreads(); }
    if (threadIdx.x >= 64) return;
    u64 metric = AMP_MEAN ? r2[0] / (cn[0] ? cn[0] : 1) - (r0[0] & 0xffffffffffull) : r1[0] - r0[0];
    if (metric > 4000) metric = 4000;
    const u64 now = __builtin_amdgcn_s_memrealtime();
    while (__builtin_amdgcn_s_memrealtime() < now + metric * 100) __builtin_amdgcn_s_sleep(8);
}

extern "C" void kernel_launch(void* const* d_in, const int* in_sizes, int n_in, void* d_out, int out_size, void* d_ws, size_t ws_size, hipStream_t stream) {
    gfnet_mod_kernel<<<256, 512, 0, stream>>>((const char*)d_in[0], (const char*)d_in[9], (const char*)d_in[8], (char*)d_ws, (const char*)d_in[1], (const char*)d_in[2],
                                              (const char*)d_in[4], (char*)d_out, (const char*)d_in[5], (const char*)d_in[6], (const char*)d_in[7]);
    k_amp<<<1, 1024, 0, stream>>>((const char*)d_ws);
}
```

```cpp
#define D_X1 8
#define D_X2 18
#include <hip/hip_runtime.h>
#include <stdint.h>
#pragma clang diagnostic ignored "-Wint-to-pointer-cast"

typedef _Float16 h2 __attribute__((ext_vector_type(2)));
typedef _Float16 h8 __attribute__((ext_vector_type(8)));
typedef __bf16 b2 __attribute__((ext_vector_type(2)));
typedef __bf16 b8 __attribute__((ext_vector_type(8)));
typedef short s4 __attribute__((ext_vector_type(4)));
typedef float f2 __attribute__((ext_vector_type(2)));
typedef float f4 __attribute__((ext_vector_type(4)));
typedef unsigned u2 __attribute__((ext_vector_type(2)));
typedef unsigned u4 __attribute__((ext_vector_type(4)));
typedef unsigned long long ul2 __attribute__((ext_vector_type(2)));
typedef unsigned long long ul4 __attribute__((ext_vector_type(4)));
typedef __amdgpu_buffer_rsrc_t rsrc_t;
#define LAS __attribute__((address_space(3)))
#define DEV __device__ __forceinline__

#define STAMP 0
#define D_CA 0
#define D_W 2
#define D_KEY 2
#define D_KEYB 10
#define D_YP 8
#define D_CAN 0
#define D_PFM 12
#define D_PFD 2
#define D_PFW 0
#define OUTAUX 0
#define PF_WG 4

constexpr int WS_EPOCH = 0;
constexpr int WS_BASE = 1024;
constexpr int WS_KEY = 4096;
constexpr int WS_CA = WS_KEY + 32768;
constexpr int WS_SA = WS_CA + 81920;
constexpr int WS_MODR = WS_SA + 81920;
constexpr int WS_YP = 262144;
constexpr int WS_ROW = WS_YP + 245760;
constexpr int XSTR = 16640;
constexpr int WS_X = 1 << 20;
constexpr int WS_END = WS_X + 162 * XSTR;
constexpr int WS_DBG = 8 << 20;
static_assert(WS_MODR + 40960 <= WS_YP && WS_ROW + 524288 <= WS_X && WS_END < WS_DBG, "workspace map");

constexpr int L_N = 0, NP = 1312;
constexpr int L_CA = 10496, CAP = 144;
constexpr int L_S = L_CA + 2 * 2304;
constexpr int L_SU = L_S + 1536;
constexpr int L_MISC = L_SU + 512;
constexpr int L_H = L_MISC + 1024;
constexpr int L_C3 = L_H + 1536;
constexpr int L_MOD = L_C3 + 4608, MP = 3072;
constexpr int L_TW = L_MOD, TWP = 80, TWQ = 3904;
constexpr int L_E3 = L_MOD + 7 * MP, E3P = 144;
constexpr int L_UT = L_E3;
constexpr int L_SINK = L_E3 + 128 * E3P;
constexpr int L_E1 = L_SINK + 256, E1P = 1040;
constexpr int L_E2 = L_E1 + 16640, E2P = 4128;
constexpr int L_MT = L_E1;
constexpr int L_GU = L_MT + 40960;
constexpr int L_END = L_GU + 40960;
static_assert(L_E2 + 4 * E2P <= L_GU && L_E1 % 16 == 0 && L_E3 % 16 == 0 && L_END + 2048 <= 163840, "LDS map");
constexpr int M_STAT = L_MISC, M_WIN = L_MISC + 128, M_US = L_MISC + 144, M_PART = L_MISC + 256;

constexpr int OOB = 0x7ffffff0;
constexpr unsigned SENT = 0xffffffffu, LSB2 = 0x00010001u;
constexpr int SPIN = 1 << 15;
constexpr int SC1 = 16;
constexpr int W_SA = 90;
constexpr float RZ = 1.00034f;

DEV h2 as_h2(unsigned v) { return __builtin_bit_cast(h2, v); }
DEV b2 as_b2(unsigned v) { return __builtin_bit_cast(b2, v); }
DEV unsigned as_u(h2 v) { return __builtin_bit_cast(unsigned, v); }
DEV unsigned as_u(float v) { return __builtin_bit_cast(unsigned, v); }
DEV float as_f(unsigned v) { return __builtin_bit_cast(float, v); }
DEV f4 as_f4(u4 v) { return __builtin_bit_cast(f4, v); }
DEV unsigned pkh(float a, float b) { f2 p = {a, b}; return as_u(__builtin_convertvector(p, h2)); }
DEV unsigned pkb(float a, float b) { f2 p = {a, b}; return __builtin_bit_cast(unsigned, __builtin_convertvector(p, b2)); }
DEV float bflo(unsigned v) { return as_f(v << 16); }
DEV float bfhi(unsigned v) { return as_f(v & 0xffff0000u); }
DEV f4 unb(u2 v) { const unsigned a = v[0], b = v[1]; f4 r = {bflo(a), bfhi(a), bflo(b), bfhi(b)}; return r; }
DEV u2 pkb4(f4 v) { return u2{pkb(v[0], v[1]), pkb(v[2], v[3])}; }
DEV u2 pkh4(f4 v) { return u2{pkh(v[0], v[1]), pkh(v[2], v[3])}; }
DEV f4 mfma(u4 a, u4 b, f4 c) { return __builtin_amdgcn_mfma_f32_16x16x32_f16(__builtin_bit_cast(h8, a), __builtin_bit_cast(h8, b), c, 0, 0, 0); }
DEV f4 mfmab(u4 a, u4 b, f4 c) { return __builtin_amdgcn_mfma_f32_16x16x32_bf16(__builtin_bit_cast(b8, a), __builtin_bit_cast(b8, b), c, 0, 0, 0); }
DEV u4 pack4(f4 re, f4 im) { u4 r = {pkh(re[0], im[0]), pkh(re[1], im[1]), pkh(re[2], im[2]), pkh(re[3], im[3])}; return r; }
DEV u4 cmul_pack(f4 re, f4 im, f4 wr, f4 wi) { return pack4(re * wr - im * wi, re * wi + im * wr); }
DEV rsrc_t mkrs(const void* p, int n) { return __builtin_amdgcn_make_buffer_rsrc((void*)p, 0, n, 0x00020000); }
template <int AUX> DEV u4 ld4(rsrc_t r, int o, int so = 0) { return __builtin_bit_cast(u4, __builtin_amdgcn_raw_buffer_load_b128(r, o, so, AUX)); }
template <int AUX> DEV u2 ld2(rsrc_t r, int o, int so = 0) { return __builtin_bit_cast(u2, __builtin_amdgcn_raw_buffer_load_b64(r, o, so, AUX)); }
template <int AUX> DEV unsigned ld1(rsrc_t r, int o) { return __builtin_amdgcn_raw_buffer_load_b32(r, o, 0, AUX); }
template <int AUX> DEV void st4(u4 v, rsrc_t r, int o) { __builtin_amdgcn_raw_buffer_store_b128(v, r, o, 0, AUX); }
template <int AUX> DEV void st2(u2 v, rsrc_t r, int o, int so = 0) { __builtin_amdgcn_raw_buffer_store_b64(v, r, o, so, AUX); }
template <int AUX> DEV void st1(unsigned v, rsrc_t r, int o) { __builtin_amdgcn_raw_buffer_store_b32(v, r, o, 0, AUX); }
template <class T> DEV T lds_ld(int a) { return *(const LAS T*)a; }
template <class T> DEV void lds_st(int a, T v) { *(LAS T*)a = v; }
DEV u2 lds_tr(int a) { return __builtin_bit_cast(u2, __builtin_amdgcn_ds_read_tr16_b64_v4i16((LAS s4*)a)); }
DEV u4 lds_tr2(int a0, int a1) { const u2 x = lds_tr(a0), y = lds_tr(a1); u4 r = {x[0], x[1], y[0], y[1]}; return r; }
DEV void dma16(rsrc_t r, int voff, int soff, int ldsbase) { asm volatile("s_mov_b32 m0, %3\n\ts_nop 0\n\tbuffer_load_dwordx4 %0, %1, %2 offen lds" :: "v"(voff), "s"(r), "s"(soff), "s"(ldsbase) : "memory"); }
DEV void dma4(rsrc_t r, int voff, int ldsbase) { asm volatile("s_mov_b32 m0, %2\n\ts_nop 0\n\tbuffer_load_dword %0, %1, 0 offen lds" :: "v"(voff), "s"(r), "s"(ldsbase) : "memory"); }
#define DRAIN() asm volatile("s_waitcnt vmcnt(0)" ::: "memory")
#define BAR() asm volatile("s_waitcnt lgkmcnt(0)\n\ts_barrier" ::: "memory")
#define FENCE() asm volatile("" ::: "memory")
#define SB() __builtin_amdgcn_sched_barrier(0)
#define PIN(x) asm volatile("" : "+v"(x))
#define PRIO(n) __builtin_amdgcn_s_setprio(n)
template <int N> DEV void nap() { if (N > 0) __builtin_amdgcn_s_sleep(N); }
DEV bool any(bool b) { return __builtin_amdgcn_ballot_w64(b) != 0; }
template <int CTRL> DEV unsigned dpp(unsigned v) { return (unsigned)__builtin_amdgcn_update_dpp(0, (int)v, CTRL, 0xf, 0xf, true); }
template <int CTRL> DEV float dppf(float v) { return as_f(dpp<CTRL>(as_u(v))); }
DEV unsigned rdl(unsigned v, int lane) { return (unsigned)__builtin_amdgcn_readlane((int)v, lane); }
DEV float row_sum(float v) { v += dppf<0x128>(v); v += dppf<0x124>(v); v += dppf<0x122>(v); v += dppf<0x121>(v); return v; }
DEV float fold4(float a, float b, float c, float d) {
    const auto p = __builtin_amdgcn_permlane32_swap(as_u(a), as_u(c), false, false);
    const auto q = __builtin_amdgcn_permlane32_swap(as_u(b), as_u(d), false, false);
    const auto s = __builtin_amdgcn_permlane16_swap(as_u(as_f(p[0]) + as_f(p[1])), as_u(as_f(q[0]) + as_f(q[1])), false, false);
    return as_f(s[0]) + as_f(s[1]);
}
DEV float wave_sum(float v) {
    v = row_sum(v);
    return (as_f(rdl(as_u(v), 0)) + as_f(rdl(as_u(v), 16))) + (as_f(rdl(as_u(v), 32)) + as_f(rdl(as_u(v), 48)));
}
DEV unsigned wave_umax(unsigned v) {
    v = max(v, dpp<0x128>(v)); v = max(v, dpp<0x124>(v)); v = max(v, dpp<0x122>(v)); v = max(v, dpp<0x121>(v));
    return max(max(rdl(v, 0), rdl(v, 16)), max(rdl(v, 32), rdl(v, 48)));
}
DEV f2 lo2(f4 v) { return f2{v[0], v[1]}; }
DEV f2 hi2(f4 v) { return f2{v[2], v[3]}; }
DEV float hsum(f4 v) { const f2 t = lo2(v) + hi2(v); return t[0] + t[1]; }
DEV float silu(float v) { return v * __builtin_amdgcn_rcpf(1.f + __expf(-v)); }
DEV float cosr(float rev) { return __builtin_amdgcn_cosf(rev); }
DEV float sinr(float rev) { return __builtin_amdgcn_sinf(rev); }
DEV f2 cis(float rev) { return f2{cosr(rev), sinr(rev)}; }
DEV unsigned pkz(float a, float b) { return __builtin_bit_cast(unsigned, __builtin_amdgcn_cvt_pkrtz(a, b)); }
DEV u4 pack4z(f4 re, f4 im) { u4 r = {pkz(re[0], im[0]), pkz(re[1], im[1]), pkz(re[2], im[2]), pkz(re[3], im[3])}; return r; }
DEV unsigned orx(unsigned a, unsigned b, unsigned c) { return __builtin_amdgcn_bitop3_b32(a, b, c, 0xf6); }
DEV unsigned andor(unsigned a, unsigned b, unsigned c) { return __builtin_amdgcn_bitop3_b32(a, b, c, 0xea); }
DEV unsigned tmul(unsigned v, unsigned w) { const h2 b = as_h2(w); return as_u(as_h2(v) * h2{b[0], b[0]}); }
template <bool CONJ> DEV unsigned tfma(unsigned v, unsigned w, unsigned t) {
    const h2 a = as_h2(v), b = as_h2(w);
    return as_u(__builtin_elementwise_fma(h2{a[1], a[0]}, CONJ ? h2{b[1], -b[1]} : h2{-b[1], b[1]}, as_h2(t)));
}
#define UNLIKELY(x) __builtin_expect(!!(x), 0)
template <int N> DEV void run(f2 (&v)[N], f2 v0, f2 v1, float t) {
    v[0] = v0; v[1] = v1;
#pragma unroll
    for (int n = 2; n < N; ++n) v[n] = t * v[n - 1] - v[n - 2];
}
DEV int col_kappa(int q, int cc) { const int pi = cc >> 1, m = pi == 0 ? 20 : 40 - pi; return (q != 0 && (cc & 1)) ? m : pi; }
DEV int col_k1(int q, int cc) { return q == 0 ? 4 * (cc & 1) : q; }
DEV int piece_seg(int q, int cc, int g) {
    const int a = (2 * q + (g >> 1)) * 20 + (g & 1) * 10 + (cc >> 2), b = (cc & 1) * 20 + g * 5 + (cc >> 3);
    return cc >= 40 ? 160 + (cc & 1) : (q ? a : b);
}
DEV int piece_idx(int q, int cc, int g) { return cc >= 40 ? g : (q ? cc & 3 : (cc >> 1) & 3); }
DEV int piece_off(int q, int cc, int g, int p, bool on) { return on ? WS_X + piece_seg(q, cc, g) * XSTR + p * 64 + piece_idx(q, cc, g) * 16 : OOB; }
DEV void gemv_tile(int img, int sv, int l, f4& a0, f4& a1) {
    const int g = l >> 4, ta = img + (8 * g + ((l >> 2) & 3)) * 64 + (l & 3) * 8;
    u4 sf[5], b0[5], b1[5];
#pragma unroll
    for (int s = 0; s < 5; ++s) {
        sf[s] = lds_ld<u4>(sv + s * 64 + g * 16);
        b0[s] = lds_tr2(ta + s * 2048, ta + s * 2048 + 256); b1[s] = lds_tr2(ta + s * 2048 + 32, ta + s * 2048 + 288);
    }
    SB();
    a0 = f4{0, 0, 0, 0}; a1 = a0;
#pragma unroll
    for (int s = 0; s < 5; ++s) { a0 = mfmab(sf[s], b0[s], a0); a1 = mfmab(sf[s], b1[s], a1); }
}

#define TS(i)
#define TSINIT()
#define TSDUMP()

__global__ void __launch_bounds__(512, 2)
gfnet_mod_kernel(const char* a_x, const char* a_wmod, const char* a_c, char* a_ws, const char* a_wr, const char* a_wi, const char* a_br, char* a_out,
                 const char* a_gate, const char* a_up, const char* a_down) {
    __shared__ __attribute__((aligned(16))) char lds_image[L_END + STAMP * 2048];
    asm volatile("" :: "s"((LAS char*)lds_image));
    ul4 ka, kb; ul2 kc; unsigned long long kd_;
    asm volatile("s_load_dwordx8 %0, %4, 0x0\n\ts_load_dwordx8 %1, %4, 0x20\n\ts_load_dwordx4 %2, %4, 0x40\n\ts_load_dwordx2 %3, %4, 0x50"
                 : "=&s"(ka), "=&s"(kb), "=&s"(kc), "=&s"(kd_) : "s"(__builtin_amdgcn_kernarg_segment_ptr()));
    const int w = blockIdx.x, x8 = w & 7, r = w >> 3, s2 = 32 * x8 + r;
    const int tid = threadIdx.x, wave = __builtin_amdgcn_readfirstlane(tid >> 6), l = tid & 63, g = l >> 4, c = l & 15;
    {
        unsigned long long pc; int len;
        asm volatile("s_getpc_b64 %0\n.Lgf_pc:\n\ts_mov_b32 %1, .Lgf_end-.Lgf_pc" : "=s"(pc), "=s"(len));
        dma4(mkrs((const void*)pc, r < PF_WG ? len : 0), tid * 128, L_SINK);
    }
    const bool lo = wave < 4;
    const int hidx = tid - 320, wq = wave - 4, rb = r - 12;
    const int ukq = x8 >> 1, ucg = (x8 & 1) * 10 + (r < 10 ? r : rb);
    const bool urgent = (wave == 4) & ((r < 10) | ((unsigned)rb < 10u));
    int ooff = 32 * c + 8 * g;
    int o2 = c < 8 ? 1024 + ooff : OOB;
    int rowb = (256 * wave + s2) * 1280;
    int uo = (l >> 2) * 7680 + (l & 3) * 16;
    int ub = ukq * 160 * 7680 + (r < 10 ? 1280 : 0) + ucg * 64;
    int cqo = tid * 8 + (wave == 4 ? ukq * 320 - 2048 : -2560);
    PIN(uo); PIN(cqo); PIN(ooff); PIN(o2);
    asm volatile("s_waitcnt lgkmcnt(0)" : "+s"(ka), "+s"(kb), "+s"(kc), "+s"(kd_), "+s"(rowb), "+s"(ub));
    const char *xin = (const char*)ka[0], *wmod = (const char*)ka[1], *cvec = (const char*)ka[2], *awr = (const char*)kb[0], *awi = (const char*)kb[1], *bwr = (const char*)kb[2];
    const char *wgate = (const char*)kc[0], *wup = (const char*)kc[1], *wdown = (const char*)kd_;
    char *ws = (char*)ka[3], *out = (char*)kb[3];
    const rsrc_t rw = mkrs(ws, WS_END), rx = mkrs(xin, 2048 * 1280), rm = mkrs(wmod, 640 * 7680), rc = mkrs(cvec, 1280);

    u2 cq;
    asm volatile("buffer_load_dwordx2 %0, %1, %2, 0 offen" : "=v"(cq) : "v"(cqo), "s"(rc));
    if (urgent) {
        PRIO(3);
#pragma clang loop unroll(disable)
        for (int i = 0; i < 10; ++i) dma16(rm, uo, ub + i * 16 * 7680, L_UT + i * 1024);
    }
    const unsigned epoch = ld1<SC1>(rw, WS_EPOCH);
    const u2 xr0 = ld2<0>(rx, ooff, rowb), xr1 = ld2<0>(rx, 512 + ooff, rowb), xr2 = ld2<0>(rx, o2, rowb);
    TS(23);

    const bool dual = r == 11 && x8 < 2, roleB = r >= 12 || dual, roleF = r < 12, roleM = r < 10;
    const int seg = dual ? 160 + x8 : 20 * x8 + rb, ft = 12 * x8 + r;
    const f4 zero4 = {0, 0, 0, 0};
    int nw = L_N + wave * NP + ooff, nw2 = c < 8 ? nw + 1024 : L_SINK;
    PIN(nw); PIN(nw2);
    unsigned tag; u4 xp;
    f4 xc[3]; float m1;
    auto ln1 = [&]() {
        m1 = as_f((unsigned)__builtin_amdgcn_readfirstlane((int)(xr0[0] << 16)));
        xc[0] = unb(xr0) - m1; xc[1] = unb(xr1) - m1; xc[2] = unb(xr2) - (c < 8 ? m1 : 0.f);
        const f4 a = xc[0] + xc[1] + xc[2], b = xc[0] * xc[0] + xc[1] * xc[1] + xc[2] * xc[2];
        const f2 a2 = lo2(a) + hi2(a), b2v = lo2(b) + hi2(b);
        const unsigned t = as_u(row_sum(fold4(a2[0], a2[1], b2v[0], b2v[1])));
        const float m = (as_f(rdl(t, 0)) + as_f(rdl(t, 16))) * (1.f / 640.f), q = (as_f(rdl(t, 32)) + as_f(rdl(t, 48))) * (1.f / 640.f);
        const float rs = __builtin_amdgcn_rsqf(fmaxf(q - m * m, 0.f) + 1e-5f), o = -m * rs;
        lds_st<u2>(nw, pkh4(xc[0] * rs + o)); lds_st<u2>(nw + 512, pkh4(xc[1] * rs + o)); lds_st<u2>(nw2, pkh4(xc[2] * rs + o));
        TS(2);
        BAR();
        TS(3);
    };
    auto settag = [&]() {
        tag = (unsigned)__builtin_amdgcn_readfirstlane((int)epoch) + 1u;
        const unsigned t = tag | (tag << 15);
        xp = u4{((tag & 1u) | ((tag & 2u) << 15)), ((tag & 1u) | ((tag & 2u) << 15)) ^ LSB2, (t >> 2) & LSB2, (t >> 4) & LSB2};
    };
    u4 c3[3], c3h[3]; f4 vga[3], vu[3], vg1[3], vg0[3]; float usum, rconst;
    auto modread = [&]() {
#pragma unroll
        for (int T = 0; T < 3; ++T) {
            const int o = L_MOD + T * 1024 + l * 16;
            vga[T] = lds_ld<f4>(o); vu[T] = lds_ld<f4>(o + MP); vg1[T] = lds_ld<f4>(o + 4 * MP); vg0[T] = lds_ld<f4>(o + 5 * MP) + m1;
            c3[T] = lds_ld<u4>(L_C3 + T * 1536 + l * 16);
            const u2 h = lds_ld<u2>(L_C3 + T * 1536 + 1024 + l * 8);
            c3h[T] = u4{h[0], h[1], 0, 0};
        }
        const f4 us = lds_ld<f4>(M_US) + lds_ld<f4>(M_US + 16) + lds_ld<f4>(M_US + 32);
        usum = us[0] + us[1]; rconst = us[2] + us[3];
#pragma unroll
        for (int T = 0; T < 3; ++T) { PIN(vga[T]); PIN(vg1[T]); PIN(vg0[T]); PIN(vu[T]); PIN(c3[T]); PIN(c3h[T]); }
        PIN(usum); PIN(rconst);
    };

    if (lo) {
        u4 a1r[3][2], a1i[3][2], a1h[3], fre, fim, atw[3]; int xo[3];
        lds_st<u4>(L_CA + tid * 16, u4{SENT, SENT, SENT, SENT});
        if (tid < 32) lds_st<u4>(L_CA + 4096 + tid * 16, u4{SENT, SENT, SENT, SENT});
        const float ma = (wave == 0 && (c & 1)) ? 0.f : 1.f;
        f2 mA = {ma, 1.f - ma}, mB = {0.f, wave ? 1.f : 0.f};
        PIN(mA); PIN(mB);
        const float ph = (float)(s2 * col_k1(wave, c)) * (1.f / 2048.f);
#pragma unroll
        for (int T = 0; T < 3; ++T) {
            const int cc = 16 * T + c;
            const float kap = (float)col_kappa(wave, cc), th0 = (float)g * kap * 0.25f, st = kap * (1.f / 40.f), t = 2.f * cosr(st);
            const f2 p = cis(th0), q = cis(th0 + st);
            f2 vr[10], vi[10];
            run<10>(vr, p[0] * mA + p[1] * mB, q[0] * mA + q[1] * mB, t);
            run<10>(vi, p[0] * mB - p[1] * mA, q[0] * mB - q[1] * mA, t);
#pragma unroll
            for (int i = 0; i < 10; ++i) {
                const unsigned re = pkh(vr[i][0], vr[i][1]), im = pkh(vi[i][0], vi[i][1]);
                if (i < 8) { a1r[T][i >> 2][i & 3] = re; a1i[T][i >> 2][i & 3] = im; } else { a1h[T][i - 8] = re; a1h[T][i - 6] = im; }
            }
            const float k640 = kap * (1.f / 640.f);
#pragma unroll
            for (int i = 0; i < 4; ++i) { const float th = -((float)(4 * g + i) * k640 + ph); atw[T][i] = pkh(cosr(th), sinr(th)); }
            lds_st<u4>(L_TW + wave * TWQ + cc * TWP + g * 16, atw[T]);
        }
        xo[0] = piece_off(wave, c, g, s2, true); xo[1] = xo[0] + (wave ? 4 : 2) * XSTR;
        xo[2] = c < 8 ? xo[0] + (wave ? 8 : 4) * XSTR : ((wave == 0 && c < 10) ? WS_X + (152 + c) * XSTR + s2 * 64 + g * 16 : OOB);
#pragma unroll
        for (int i = 0; i < 4; ++i) { const f2 v = cis((float)((4 * g + i) * c) * (1.f / 16.f)) * 0.25f; fre[i] = pkh(v[0], v[1]); fim[i] = pkh(-v[1], v[0]); }
#pragma unroll
        for (int T = 0; T < 3; ++T) { PIN(a1r[T][0]); PIN(a1r[T][1]); PIN(a1i[T][0]); PIN(a1i[T][1]); PIN(a1h[T]); PIN(atw[T]); PIN(xo[T]); }
        PIN(fre); PIN(fim);
        constexpr unsigned P = 0x3800, M = 0xb800, p = 0x35a8, m = 0xb5a8;
        const unsigned gs = (wave & 1) ? 0xbc00bc00u : 0x3c003c00u;
        const u4 gc = wave == 0 ? u4{P | P << 16, P | M << 16, P | P << 16, P | M << 16} : wave == 1 ? u4{P, p | m << 16, M << 16, m | m << 16}
                    : wave == 2 ? u4{P, M << 16, M, P << 16} : u4{P, m | m << 16, P << 16, p | m << 16};
        int go = tid < 160 ? WS_CA + x8 * 10240 + tid * 16 : OOB;
        int cw = L_CA + (tid & 7) * 2 * CAP + ((tid >> 3) / 5) * 32 + ((tid >> 3) % 5) * 4, cr = L_CA + c * CAP + g * 32;
        int na = L_N + ((l >> 2) & 3) * NP + (l & 3) * 8 + 320 * g;
        unsigned nl = ~LSB2, gm = tid < 160 ? ~0u : 0u;
        if (tid >= 160) cw = L_SINK;
        PIN(go); PIN(cw); PIN(cr); PIN(na); PIN(nl); PIN(gm);
        TS(1);
        settag();
        ln1();
        asm volatile("" :: "v"(cq));
        PRIO(2);
        nap<D_CA>();
        u4 gv[4];
        if (wave < 3) {
#pragma unroll
            for (int k = 0; k < 4; ++k) gv[k] = ld4<SC1>(rw, go, k * 2560);
        }
        unsigned y[10];
        {
            u4 v[10];
#pragma unroll
            for (int i = 0; i < 10; ++i) v[i] = lds_tr2(na + 32 * i, na + 32 * i + 4 * NP);
            SB();
#pragma unroll
            for (int i = 0; i < 10; ++i) {
                const h2 a = __builtin_elementwise_fma(as_h2(v[i][2]), as_h2(gs), as_h2(v[i][0])), b = __builtin_elementwise_fma(as_h2(v[i][3]), as_h2(gs), as_h2(v[i][1]));
                h2 t = h2{a[0], a[0]} * as_h2(gc[0]);
                t = __builtin_elementwise_fma(h2{a[1], a[1]}, as_h2(gc[1]), t); t = __builtin_elementwise_fma(h2{b[0], b[0]}, as_h2(gc[2]), t);
                y[i] = as_u(__builtin_elementwise_fma(h2{b[1], b[1]}, as_h2(gc[3]), t));
            }
#pragma unroll
            for (int i = 0; i < 10; ++i) PIN(y[i]);
        }
        unsigned y8 = y[8], y9 = y[9], spin_t; int spin_n; u4 d2r = {0, 0, 0, 0}, d2i = {0, 0, 0, 0};
        PIN(y8); PIN(y9); PIN(d2r); PIN(d2i);
        TS(4);
        unsigned addc = 0x30003000u; bool again = w == W_SA;
        PIN(addc);
#pragma clang loop unroll(disable)
        for (;;) {
            PIN(atw[0]); PIN(atw[1]); PIN(atw[2]);
            if (wave < 3) {
                auto gbad = [&]() { return ((orx(orx(orx(gv[0][1] ^ tag, gv[1][1], tag), gv[2][1], tag), gv[3][1], tag) | orx(orx(orx(gv[0][3] ^ tag, gv[1][3], tag), gv[2][3], tag), gv[3][3], tag)) & gm) != 0; };
                if (UNLIKELY(any(gbad()))) {
                    int n = 0; bool bad = gbad();
#pragma clang loop unroll(disable)
                    do {
                        FENCE();
                        if (bad) {
#pragma unroll
                            for (int k = 0; k < 4; ++k) gv[k] = ld4<SC1>(rw, go, k * 2560);
                        }
                        bad = gbad();
                    } while (any(bad) && ++n < SPIN);
                }
                const h2 e8 = as_h2(0x30003000u);
                const h2 s0 = ((as_h2(gv[0][0]) + as_h2(gv[1][0])) + (as_h2(gv[2][0]) + as_h2(gv[3][0]))) * e8 + as_h2(addc);
                const h2 s1 = ((as_h2(gv[0][2]) + as_h2(gv[1][2])) + (as_h2(gv[2][2]) + as_h2(gv[3][2]))) * e8 + as_h2(addc);
                lds_st<unsigned>(cw, as_u(s0)); lds_st<unsigned>(cw + CAP, as_u(s1));
            }
            u4 ca; unsigned cb;
            asm volatile("s_mov_b32 %3, 0x80000\n"
                         ".Lgf_spin%=:\n\t"
                         "ds_read_b32 %2, %4\n\tds_read_b32 %1, %4 offset:16\n\tds_read_b128 %0, %4\n\ts_sub_u32 %3, %3, 1\n\ts_cbranch_scc1 .Lgf_spun%=\n\t"
                         "s_waitcnt lgkmcnt(1)\n\tv_max_u32 %2, %2, %1\n\tv_cmp_eq_u32 vcc, -1, %2\n\ts_cbranch_vccnz .Lgf_spin%=\n"
                         ".Lgf_spun%=:\n\ts_waitcnt lgkmcnt(0)"
                         : "=&v"(ca), "=&v"(cb), "=&v"(spin_t), "=&s"(spin_n) : "v"(cr) : "vcc", "scc", "memory");
            SB();
            TS(5);
            {
                unsigned z[10]; f4 re[3], im[3], ore[3], oim[3]; u4 vv[3], tt[3], oo[3];
                const unsigned cv[5] = {ca[0], ca[1], ca[2], ca[3], cb};
#define Z(i) { const h2 t_ = as_h2(cv[(i) >> 1]); const _Float16 s_ = t_[(i) & 1]; z[i] = as_u(as_h2(y[i]) * h2{s_, s_}); }
#define D0 u4{z[0], z[1], z[2], z[3]}
#define D1 u4{z[4], z[5], z[6], z[7]}
#define R0(T) re[T] = mfma(D0, a1r[T][0], zero4);
#define I0(T) im[T] = mfma(D0, a1i[T][0], zero4);
#define R1(T) re[T] = mfma(D1, a1r[T][1], re[T]);
#define I1(T) im[T] = mfma(D1, a1i[T][1], im[T]);
#define R2(T) re[T] = mfma(d2r, a1h[T], re[T]);
#define I2(T) im[T] = mfma(d2i, a1h[T], im[T]);
#define CV(T, i) vv[T][i] = pkh(re[T][i], im[T][i]);
#define TM(T, i) tt[T][i] = tmul(vv[T][i], atw[T][i]);
#define TF(T, i) tt[T][i] = tfma<false>(vv[T][i], atw[T][i], tt[T][i]);
#define FR(T) ore[T] = mfma(fre, tt[T], zero4);
#define FI(T) oim[T] = mfma(fim, tt[T], zero4);
#define PK(T, i) oo[T][i] = pkz(ore[T][i], oim[T][i]);
#define TG(T, i) oo[T][i] = andor(oo[T][i], nl, xp[i]);
#define ST(T) st4<SC1>(oo[T], rw, xo[T]);
                Z(0) Z(1) Z(2) Z(3) Z(4) Z(5) SB();
                R0(0) SB(); Z(6) Z(7) SB(); I0(0) SB(); Z(8) Z(9) d2r[0] = z[8]; d2r[1] = z[9]; SB();
                R1(0) SB(); { const h2 t_ = as_h2(cb); d2i[2] = as_u(h2{t_[0], t_[0]} * as_h2(y8)); d2i[3] = as_u(h2{t_[1], t_[1]} * as_h2(y9)); } SB();
                I1(0) SB(); R2(0) SB(); I2(0) SB(); R0(1) SB(); I0(1) SB();
                R1(1) SB(); CV(0, 0) CV(0, 1) SB(); I1(1) SB(); CV(0, 2) CV(0, 3) SB(); R2(1) SB(); TM(0, 0) TM(0, 1) SB(); I2(1) SB(); TM(0, 2) TM(0, 3) SB();
                R0(2) SB(); TF(0, 0) TF(0, 1) SB(); I0(2) SB(); TF(0, 2) TF(0, 3) SB(); R1(2) SB(); CV(1, 0) CV(1, 1) SB(); I1(2) SB(); CV(1, 2) CV(1, 3) SB();
                R2(2) SB(); TM(1, 0) TM(1, 1) SB(); I2(2) SB(); TM(1, 2) TM(1, 3) SB(); FR(0) SB(); TF(1, 0) TF(1, 1) SB(); FI(0) SB(); TF(1, 2) TF(1, 3) SB();
                CV(2, 0) CV(2, 1) SB(); FR(1) SB(); CV(2, 2) CV(2, 3) SB(); FI(1) SB(); TM(2, 0) TM(2, 1) TM(2, 2) TM(2, 3) SB(); TF(2, 0) TF(2, 1) TF(2, 2) TF(2, 3) SB();
                PK(0, 0) PK(0, 1) SB(); FR(2) SB(); PK(0, 2) PK(0, 3) SB(); FI(2) SB(); TG(0, 0) TG(0, 1) TG(0, 2) TG(0, 3) SB(); ST(0) SB();
                PK(1, 0) PK(1, 1) PK(1, 2) PK(1, 3) SB(); TG(1, 0) TG(1, 1) TG(1, 2) TG(1, 3) SB(); ST(1) SB();
                PK(2, 0) PK(2, 1) PK(2, 2) PK(2, 3) SB(); TG(2, 0) TG(2, 1) TG(2, 2) TG(2, 3) SB(); ST(2) SB();
#undef Z
#undef D0
#undef D1
#undef R0
#undef I0
#undef R1
#undef I1
#undef R2
#undef I2
#undef CV
#undef TM
#undef TF
#undef FR
#undef FI
#undef PK
#undef TG
#undef ST
            }
            if (!UNLIKELY(again)) break;
            again = false;
            go = tid < 160 ? WS_SA + tid * 16 : OOB; cw += tid < 160 ? 2304 : 0; cr += 2304; addc = 0;
#pragma unroll
            for (int k = 0; k < 4; ++k) { gv[k] = ld4<SC1>(rw, go, k * 2560); }
#pragma unroll
            for (int k = 0; k < 4; ++k) PIN(gv[k]);
#pragma unroll
            for (int i = 0; i < 10; ++i) y[i] = 0x3c00u;
            y8 = 0x3c00u; y9 = 0x3c00u;
            xo[0] = wave ? OOB : xo[0] + (256 - s2) * 64; xo[1] = wave ? OOB : xo[1] + (256 - s2) * 64; xo[2] = wave == 0 && c < 10 ? xo[2] + (256 - s2) * 64 : OOB;
        }
        TS(6);
        PRIO(1);
        u4 ctw[3], c2r, c2i, c2r0, c2i0; int co[3];
#pragma unroll
        for (int i = 0; i < 3; ++i)
#pragma unroll
            for (int e = 0; e < 4; ++e) ctw[i][e] = lds_ld<unsigned>(L_TW + g * TWQ + (12 * wave + 4 * i + e) * TWP + c * 4);
        if (!roleB) BAR();
#pragma unroll
        for (int i = 0; i < 3; ++i) {
            const int cc = 4 * (3 * wave + i) + (c & 3);
            co[i] = piece_off(c >> 2, cc, g, s2, (cc < 40) | ((cc < 42) & (c < 4) & (g < 2)));
        }
        {
            const int s1 = c >> 1, h = c & 1;
            const float th = (float)(s1 * g) * 0.125f, cs = 0.5f * cosr(th), si = 0.5f * sinr(th), sg = (s1 & 1) ? -0.5f : 0.5f;
            const unsigned r0 = pkh(cs, -si), i0 = pkh(si, cs), r1 = g ? r0 : pkh(sg, 0.f), i1 = g ? i0 ^ 0x80008000u : r1 << 16;
            c2r = h ? u4{0, 0, r0, r1} : u4{r0, r1, 0, 0}; c2i = h ? u4{0, 0, i0, i1} : u4{i0, i1, 0, 0};
            const unsigned d2 = as_u(as_h2(r0) + as_h2(r0));
            c2r0 = c2r; c2i0 = c2i;
            if (wave == 0 && !h) { c2r0[0] = g ? d2 : r0; c2r0[1] = g ? 0u : r1; c2i0[0] = 0u; c2i0[1] = g ? d2 : 0u; }
        }
        u4 gre = fre ^ 0x80000000u, gim = fim ^ 0x00008000u;
        const unsigned xq = xp[0] ^ 0x10000u;
        unsigned m1v = co[1] == OOB ? 0u : LSB2, m2v = co[2] == OOB ? 0u : LSB2;
        int e3w = L_E3 + (32 * g + c) * E3P + wave * 32;
#pragma unroll
        for (int i = 0; i < 3; ++i) { PIN(ctw[i]); PIN(co[i]); }
        PIN(c2r); PIN(c2i); PIN(c2r0); PIN(c2i0); PIN(gre); PIN(gim); PIN(m1v); PIN(m2v); PIN(e3w);
        TS(7);
        if (roleB) {
            int so = WS_X + seg * XSTR + (64 * g + 4 * wave) * 64 + c * 16;
            int ea = L_E1 + c * E1P + (16 * (g >> 1) + 4 * wave + 2 * (g & 1)) * 32, eb = L_E2 + wave * E2P + l * 8;
            PIN(so); PIN(ea); PIN(eb);
            nap<D_X1>();
            PRIO(2);
            u4 v[4], d[4];
            TS(8);
#pragma unroll
            for (int i = 0; i < 4; ++i) v[i] = ld4<SC1>(rw, so + i * 1024);
            SB();
            auto take = [&]() -> unsigned {
                unsigned b0 = 0, b1 = 0;
#pragma unroll
                for (int i = 0; i < 4; ++i) {
                    b0 = orx(b0, v[i][0], xp[0]); b1 = orx(b1, v[i][1], xp[1]); d[3][i] = v[i][3] | LSB2; d[2][i] = v[i][2] | LSB2;
                    b0 = orx(b0, v[i][2], xp[2]); b1 = orx(b1, v[i][3], xp[3]); d[1][i] = v[i][1] | LSB2; d[0][i] = v[i][0] | LSB2;
                    SB();
                }
                return (b0 | b1) & LSB2;
            };
            unsigned bad = take();
            if (UNLIKELY(any(bad != 0))) {
                int n = 0;
#pragma clang loop unroll(disable)
                do {
                    bad |= dpp<0xb1>(bad); bad |= dpp<0x4e>(bad);
                    FENCE();
                    if (bad) {
#pragma unroll
                        for (int i = 0; i < 4; ++i) v[i] = ld4<SC1>(rw, so + i * 1024);
                    }
                    bad = take();
                } while (any(bad != 0) && ++n < SPIN);
            }
            TS(9);
            f4 re[4], im[4]; u4 o[4];
#define MR(m) re[m] = mfma(d[m], fre, zero4); SB();
#define MI(m) im[m] = mfma(d[m], fim, zero4); SB();
#define CR(m) o[(m) >> 1][2 * ((m) & 1)] = pkh(re[m][0], re[m][1]); o[(m) >> 1][2 * ((m) & 1) + 1] = pkh(re[m][2], re[m][3]); SB();
#define CI(m) o[2 + ((m) >> 1)][2 * ((m) & 1)] = pkh(im[m][0], im[m][1]); o[2 + ((m) >> 1)][2 * ((m) & 1) + 1] = pkh(im[m][2], im[m][3]); SB();
#define WR(k) lds_st<u4>(ea + 16 * (k), o[k]); SB();
            SB(); MR(0) MR(1) MI(0) MI(1) CR(0) MR(2) CR(1) MR(3) WR(0) CI(0) MI(2) CI(1) MI(3) WR(2) CR(2) CR(3) WR(1) CI(2) CI(3) WR(3)
#undef MR
#undef MI
#undef CR
#undef CI
#undef WR
            BAR();
            BAR();
#pragma unroll
            for (int mu = 0; mu < 4; ++mu) d[mu] = lds_tr2(eb + mu * 1024, eb + mu * 1024 + 512);
            SB();
#define MM(m) re[m] = mfma(gre, d[m], zero4); SB();
#define MN(m) im[m] = mfma(gim, d[m], zero4); SB();
#define C0(i) o[i][0] = pkz(re[0][i], im[0][i]);
#define T0(i) o[i][0] = andor(o[i][0], nl, xq);
#define CC(m, i) o[i][m] = pkh(re[m][i], im[m][i]);
#define ST(i) st4<SC1>(o[i], rw, so + (i) * 1024); SB();
            MM(0) MN(0) MM(1) MN(1) MM(2) C0(0) C0(1) SB(); MN(2) C0(2) C0(3) SB(); MM(3) T0(0) T0(1) SB(); MN(3) T0(2) T0(3) SB();
            CC(1, 0) CC(1, 1) CC(1, 2) CC(1, 3) SB(); CC(2, 0) CC(2, 1) CC(2, 2) CC(2, 3) SB();
            CC(3, 0) CC(3, 1) SB(); ST(0) CC(3, 2) SB(); ST(1) CC(3, 3) SB(); ST(2) ST(3)
#undef MM
#undef MN
#undef C0
#undef T0
#undef CC
#undef ST
            TS(10);
        } else {
            DRAIN();
            if (roleM) BAR();
            BAR();
        }
        TS(11);
        if (roleB) nap<D_X2>();
        else {
#pragma clang loop unroll(disable)
            for (int n = 0; n < SPIN; ++n) { FENCE(); if (!any(((ld1<SC1>(rw, WS_X + (20 * x8 + r) * XSTR + s2 * 64) ^ xq) & LSB2) != 0)) break; }
            nap<D_CAN>();
        }
        u4 v[3];
        TS(12);
#pragma unroll
        for (int i = 0; i < 3; ++i) v[i] = ld4<SC1>(rw, co[i]);
        BAR();
        modread();
        PRIO(2);
        auto cbad = [&]() { return (((v[0][0] ^ xq) & LSB2) | ((v[1][0] ^ xq) & m1v) | ((v[2][0] ^ xq) & m2v)) != 0; };
        if (UNLIKELY(any(cbad()))) {
            int n = 0; bool bad = cbad();
#pragma clang loop unroll(disable)
            do {
                FENCE();
                if (bad) {
#pragma unroll
                    for (int i = 0; i < 3; ++i) v[i] = ld4<SC1>(rw, co[i]);
                }
                bad = cbad();
            } while (any(bad) && ++n < SPIN);
        }
        TS(13);
        {
            f4 re[3], im[3]; u4 vv[3], tt[3], oa, ob; u2 oc, od;
            v[0][0] |= LSB2; v[1][0] |= LSB2; v[2][0] |= LSB2;
            SB();
#define MR(i) re[i] = mfma(v[i], gre, zero4); SB();
#define MI(i) im[i] = mfma(v[i], gim, zero4); SB();
#define CV(i, e) vv[i][e] = pkh(re[i][e], im[i][e]);
#define TM(i, e) tt[i][e] = tmul(vv[i][e], ctw[i][e]);
#define TF(i, e) tt[i][e] = tfma<true>(vv[i][e], ctw[i][e], tt[i][e]);
#define NR(i) re[i] = mfma(i ? c2r : c2r0, tt[i], zero4); SB();
#define NI(i) im[i] = mfma(i ? c2i : c2i0, tt[i], zero4); SB();
#define PK(i, e) pkh(re[i][e], im[i][e])
            MR(0) MI(0) MR(1) MI(1) MR(2) CV(0, 0) CV(0, 1) SB(); MI(2) CV(0, 2) CV(0, 3) SB();
            TM(0, 0) TM(0, 1) TM(0, 2) TM(0, 3) SB(); TF(0, 0) TF(0, 1) TF(0, 2) TF(0, 3) SB();
            CV(1, 0) CV(1, 1) SB(); NR(0) CV(1, 2) CV(1, 3) SB(); NI(0) TM(1, 0) TM(1, 1) TM(1, 2) TM(1, 3) SB(); TF(1, 0) TF(1, 1) TF(1, 2) TF(1, 3) SB();
            CV(2, 0) CV(2, 1) SB(); NR(1) CV(2, 2) CV(2, 3) SB(); NI(1) TM(2, 0) TM(2, 1) TM(2, 2) TM(2, 3) SB(); TF(2, 0) TF(2, 1) TF(2, 2) TF(2, 3) SB();
            oa[0] = PK(0, 0); oa[1] = PK(0, 1); SB(); NR(2) ob[0] = PK(0, 2); ob[1] = PK(0, 3); SB(); NI(2)
            oa[2] = PK(1, 0); oa[3] = PK(1, 1); ob[2] = PK(1, 2); ob[3] = PK(1, 3); SB();
            lds_st<u4>(e3w, oa); lds_st<u4>(e3w + 16 * E3P, ob); SB();
            oc[0] = PK(2, 0); oc[1] = PK(2, 1); od[0] = PK(2, 2); od[1] = PK(2, 3); SB();
            lds_st<u2>(e3w + 16, oc); lds_st<u2>(e3w + 16 * E3P + 16, od);
#undef MR
#undef MI
#undef CV
#undef TM
#undef TF
#undef NR
#undef NI
#undef PK
        }
        PRIO(0);
    } else {
        const rsrc_t rr = mkrs(awr, 2048 * 642), ri = mkrs(awi, 2048 * 642);
        const int bq = x8 >> 1, k1 = bq ? bq : 4 * (x8 & 1);
        unsigned pf0, pf1;
        {
            const int cls = (bq && (x8 & 1)) ? 8 - bq : k1, i = l & 7, ln = l >> 3;
            const int o = ((((ln < 6 ? cls : 8 - cls) & 7) + 8 * (8 * r + i)) * 642 + (ln < 6 ? ln * 128 : (x8 & 1) * 640)) & ~3;
            if (wave == 5) nap<D_PFW>();
            pf0 = ld1<0>(rr, (unsigned)hidx < 56u ? o : OOB); pf1 = ld1<0>(ri, (unsigned)hidx < 56u ? o : OOB);
        }
        const int pmi = tid - 384, pmo = (unsigned)pmi < 100u ? (20 * r + pmi / 5) * 7680 + 2560 + 640 * x8 + (pmi % 5) * 128 : OOB;
        if (wave > 4) {
            const int T = wave - 5;
            const float a40 = (float)(16 * T + c) * (-1.f / 40.f), sc = (T < 2 || c < 8) ? 0.25f : 0.f, th0 = (float)(6 * g) * a40;
            f2 v[6]; run<6>(v, cis(th0) * sc, cis(th0 + a40) * sc, 2.f * cosr(a40));
            const unsigned z = pkh(0.5f * sc, (c & 1) ? -0.5f * sc : 0.5f * sc), f = pkh(v[0][0], v[0][1]);
            lds_st<u4>(L_C3 + T * 1536 + l * 16, u4{g == 0 ? z : f, pkh(v[1][0], v[1][1]), pkh(v[2][0], v[2][1]), pkh(v[3][0], v[3][1])});
            lds_st<u2>(L_C3 + T * 1536 + 1024 + l * 8, u2{pkh(v[4][0], v[4][1]), pkh(v[5][0], v[5][1])});
        }
        if (wave > 5) nap<D_PFM>();
        const unsigned pf2 = ld1<0>(rm, pmo);
        if (urgent) asm volatile("s_waitcnt vmcnt(17)" ::: "memory"); else asm volatile("s_waitcnt vmcnt(7)" ::: "memory");
        u2 cq2; asm volatile("v_mov_b64 %0, %1" : "=v"(cq2) : "v"(cq));
        const f4 cv = unb(cq2);
        lds_st<u2>(wave == 4 ? L_SU + l * 8 : L_S + hidx * 8, u2{pkb(silu(cv[0]), silu(cv[1])), pkb(silu(cv[2]), silu(cv[3]))});
        settag();
        if (urgent) {
            int o = (r < 10 ? WS_CA : WS_SA) + g * 10240 + (ukq * 20 + ucg) * 128 + c * 8;
            PIN(o);
            PRIO(3);
            asm volatile("s_waitcnt vmcnt(6)" ::: "memory");
            TS(24);
            f4 a0, a1; gemv_tile(L_UT, L_SU, l, a0, a1);
            const u2 gr = {pkh(a0[0], a1[0]), tag};
            st2<SC1>(gr, rw, o); st2<SC1>(gr, rw, o, 40960);
            TS(1);
        }
        PRIO(1);
        ln1();
        PRIO(0);
        auto ffn_issue = [&]() {
            const rsrc_t rg = mkrs(wgate, 640 * 3072), ru = mkrs(wup, 640 * 3072), rd = mkrs(wdown + ft * 20480, 20480);
            const int o = (wq * 160 + (l >> 1)) * 3072 + ft * 32 + (l & 1) * 16, od = (l >> 5) * 1280 + (wave - 5) * 512 + (l & 31) * 16;
#pragma clang loop unroll(disable)
            for (int i = 0, j = r & 3; i < 5; ++i, j = j == 4 ? 0 : j + 1) { dma16(rg, o, j * 32 * 3072, L_GU + wq * 10240 + j * 1024); dma16(ru, o, j * 32 * 3072, L_GU + wq * 10240 + 5120 + j * 1024); }
            if (wave > 4) {
#pragma clang loop unroll(disable)
                for (int i = 0; i < 8; ++i) dma16(rd, od + i * 2560, 0, L_MT + wq * 8192 + i * 1024);
            }
        };
        if (roleB) {
            const int P = c & 3, mulo = c >> 2, bg = bq ? 2 * (x8 & 1) + (rb >= 10) : rb / 5;
            const int kd = dual ? 20 + 40 * (4 * P + mulo) : (bq ? col_kappa(1, 4 * (rb % 10) + P) : 4 * (rb % 5) + P) + 40 * (4 * bg + mulo);
            const bool mir = bg >= 2, off = dual && P >= 2;
            const int col = mir ? 640 - kd : kd, ks0 = k1 + 8 * wq + 512 * g;
            const int vo = off ? OOB : ((mir ? 1568 - ks0 : ks0) * 321 + col) * 2, v0 = off ? OOB : (((mir ? -ks0 : ks0) & 2047) * 321 + col) * 2;
            const int sb = mir ? 15 * 32 * 642 : 0, ss = mir ? -32 * 642 : 32 * 642;
            nap<D_W>();
            TS(5);
            unsigned short tr[16], ti[16], er = 0, ei = 0;
            const bool edge = bq && rb == 0;
            if (edge) {
                const int ks = ks0 + 32 * c, o = ((mir ? ks : 2048 - ks) * 321 + (mir ? 320 : 0)) * 2;
                er = __builtin_amdgcn_raw_buffer_load_b16(rr, o, 0, 0); ei = __builtin_amdgcn_raw_buffer_load_b16(ri, o, 0, 0);
            }
#pragma unroll
            for (int n = 0; n < 16; ++n) {
                tr[n] = __builtin_amdgcn_raw_buffer_load_b16(rr, n ? vo : v0, n ? sb + n * ss : 0, 0);
                ti[n] = __builtin_amdgcn_raw_buffer_load_b16(ri, n ? vo : v0, n ? sb + n * ss : 0, 0);
            }
            SB();
            u4 b2r[4], b2i[4], b3r[4], b3i[4];
            {
                const float t0 = (float)(c * (wq + 64 * g)) * (1.f / 256.f), st = (float)c * (1.f / 64.f);
                f2 v3[16]; run<16>(v3, cis(t0) * (0.25f * RZ), cis(t0 + st) * (0.25f * RZ), 2.f * cosr(st));
#pragma unroll
                for (int j = 0; j < 4; ++j) {
                    const float k2 = (float)(wq + 4 * j + 16 * c) * (1.f / 256.f), b0 = (float)(4 * g) * k2;
                    f2 v2[4]; run<4>(v2, cis(b0) * (0.25f * RZ), cis(b0 + k2) * (0.25f * RZ), 2.f * cosr(k2));
#pragma unroll
                    for (int i = 0; i < 4; ++i) {
                        b2r[j][i] = pkz(v2[i][0], v2[i][1]); b2i[j][i] = pkz(-v2[i][1], v2[i][0]);
                        b3r[j][i] = pkz(v3[j + 4 * i][0], -v3[j + 4 * i][1]); b3i[j][i] = pkz(v3[j + 4 * i][1], v3[j + 4 * i][0]);
                    }
                    PIN(b2r[j]); PIN(b2i[j]); PIN(b3r[j]); PIN(b3i[j]);
                }
            }
            asm volatile("" :: "v"(pf0), "v"(pf1), "v"(pf2));
            u4 wf[4];
            const float sg = mir ? -4.f : 4.f;
#pragma unroll
            for (int n = 0; n < 16; ++n) wf[n & 3][n >> 2] = pkh(4.f * bflo(tr[n]), sg * bflo(ti[n]));
            if (edge) {
                const h2 h0 = as_h2(c == 0 ? 0x38003800u : 0x3c003c00u), h1 = as_h2(c == 0 ? 0x38003800u : 0u);
                const unsigned f = pkh(4.f * bflo(er), -sg * bflo(ei));
#define EDGE(n) wf[n & 3][n >> 2] = as_u(h0 * as_h2(wf[n & 3][n >> 2]) + h1 * as_h2(dpp<0x150 + n>(f)));
                EDGE(0) EDGE(1) EDGE(2) EDGE(3) EDGE(4) EDGE(5) EDGE(6) EDGE(7) EDGE(8) EDGE(9) EDGE(10) EDGE(11) EDGE(12) EDGE(13) EDGE(14) EDGE(15)
#undef EDGE
            }
            int ra = L_E1 + wq * E1P + l * 8, wa = L_E2 + (c >> 2) * E2P + g * 1024 + (16 * (wq >> 1) + 2 * (wq & 1)) * 32 + (c & 3) * 8;
#pragma unroll
            for (int j = 0; j < 4; ++j) PIN(wf[j]);
            TS(6);
            f4 sar = zero4, sai = zero4;
            if (x8 == 0 && wave == 4) {
                const unsigned pt = mulo & 2 ? (mulo & 1 ? xp[3] : xp[2]) : (mulo & 1 ? xp[1] : xp[0]);
                unsigned t;
#pragma clang loop unroll(disable)
                for (int n = 0; n < SPIN; ++n) { FENCE(); t = ld1<SC1>(rw, WS_X + seg * XSTR + 256 * 64 + P * 16 + mulo * 4); if (!any(((t ^ pt) & LSB2) != 0)) break; }
                const h2 th = as_h2(t | LSB2);
                if (g == 0) { sar[0] = 64.f * (float)th[0]; sai[0] = 64.f * (float)th[1]; }
            }
            PIN(sar); PIN(sai); PIN(ra); PIN(wa);
            PRIO(2);
            TS(7);
            BAR();
            TS(8);
            u4 d[4]; f4 re[4], im[4];
#pragma unroll
            for (int j = 0; j < 4; ++j) d[j] = lds_tr2(ra + 4 * j * E1P, ra + 4 * j * E1P + 512);
            SB();
            u4 vv[4]; u2 oa, ob;
#define MR(j) re[j] = mfma(b2r[j], d[j], j ? zero4 : sar); SB();
#define MI(j) im[j] = mfma(b2i[j], d[j], j ? zero4 : sai); SB();
#define CV(j, e) vv[j][e] = pkh(re[j][e], im[j][e]);
#define TM(j, e) d[j][e] = tmul(vv[j][e], wf[j][e]);
#define TF(j, e) d[j][e] = tfma<false>(vv[j][e], wf[j][e], d[j][e]);
#define NR(j) re[j] = mfma(d[j], b3r[j], zero4); SB();
#define NI(j) im[j] = mfma(d[j], b3i[j], zero4); SB();
#define PR(j) oa = pkh4(re[j]); SB();
#define PI(j) ob = pkh4(im[j]); SB();
#define WR(j) lds_st<u2>(wa + 128 * (j), oa); lds_st<u2>(wa + 128 * (j) + 32, ob); SB();
#define TM4(j) TM(j, 0) TM(j, 1) TM(j, 2) TM(j, 3) SB();
#define TF4(j) TF(j, 0) TF(j, 1) TF(j, 2) TF(j, 3) SB();
            MR(0) MI(0) MR(1) MI(1) MR(2) CV(0, 0) CV(0, 1) SB(); MI(2) CV(0, 2) CV(0, 3) SB(); MR(3) TM(0, 0) TM(0, 1) SB(); MI(3) TM(0, 2) TM(0, 3) SB(); TF4(0)
            CV(1, 0) CV(1, 1) SB(); NR(0) CV(1, 2) CV(1, 3) SB(); NI(0) TM4(1) TF4(1)
            CV(2, 0) CV(2, 1) SB(); NR(1) CV(2, 2) CV(2, 3) SB(); NI(1) TM4(2) TF4(2)
            CV(3, 0) CV(3, 1) SB(); NR(2) CV(3, 2) CV(3, 3) SB(); NI(2) TM4(3) TF4(3)
            PR(0) NR(3) PI(0) NI(3) WR(0) PR(1) PI(1) WR(1) PR(2) PI(2) WR(2) PR(3) PI(3) WR(3)
#undef MR
#undef MI
#undef CV
#undef TM
#undef TF
#undef NR
#undef NI
#undef PR
#undef PI
#undef WR
#undef TM4
#undef TF4
            BAR();
            PRIO(0);
            TS(9);
            if (w == 255 && wave == 4) st1<SC1>(tag, rw, l == 0 ? WS_EPOCH : OOB);
        } else {
            if (roleM && wave > 4) { nap<D_PFD>(); dma4(mkrs(wdown + x8 * 245760, 245760), (192 * r + hidx) * 128, L_SINK); }
            BAR();
            asm volatile("" :: "v"(pf0), "v"(pf1), "v"(pf2));
            if (roleM) {
                const int uid = 10 * x8 + r;
#pragma clang loop unroll(disable)
                for (int i = 0, j = 5 * (r & 1); i < 10; ++i, j = j == 9 ? 0 : j + 1) dma16(rm, uo, (wq * 160 + j * 16) * 7680 + 2560 + uid * 64, L_MT + wq * 10240 + j * 1024);
                const int pg = wave > 4 ? (64 * r + l) * 3072 + x8 * 384 + (wave - 5) * 128 : OOB;
                dma4(mkrs(wgate, 640 * 3072), pg, L_SINK); dma4(mkrs(wup, 640 * 3072), pg, L_SINK);
                asm volatile("s_waitcnt vmcnt(2)" ::: "memory");
                f4 a0, a1; gemv_tile(L_MT + wq * 10240, L_S + wq * 320, l, a0, a1);
                if (g < 2) lds_st<float>(M_PART + l * 16 + wq * 4, g ? a1[0] : a0[0]);
                BAR();
                if (wave == 4) {
                    const float v0 = hsum(lds_ld<f4>(M_PART + c * 32)), v1 = hsum(lds_ld<f4>(M_PART + c * 32 + 16));
                    st2<SC1>(u2{pkb(v0, v1), tag}, rw, WS_MODR + g * 10240 + uid * 128 + c * 8);
                }
            }
            TS(5);
        }
        if (wave > 4) {
            const int o = hidx < 160 ? WS_MODR + (x8 & 3) * 10240 + hidx * 16 : OOB;
            const u2 wrr = ld2<0>(mkrs(bwr, 1280), hidx * 8);
            u4 v[4];
#pragma clang loop unroll(disable)
            for (int n = 0; n < SPIN; ++n) {
                FENCE();
                unsigned d = 0;
#pragma unroll
                for (int k = 0; k < 4; ++k) v[k] = ld4<SC1>(rw, o, k * 2560);
#pragma unroll
                for (int k = 0; k < 4; ++k) d |= (v[k][1] ^ tag) | (v[k][3] ^ tag);
                if (!any(d != 0 && hidx < 160)) break;
            }
            const f4 wv = unb(wrr), sb = unb(u2{v[1][0], v[1][2]}), c1 = unb(u2{v[2][0], v[2][2]}) + 1.f, gb = unb(u2{v[3][0], v[3][2]}), u = c1 * wv, b = sb * wv;
            const int a = L_MOD + (wave - 5) * 1024 + (l & 3) * 256 + (l >> 2) * 16;
            lds_st<f4>(a, unb(u2{v[0][0], v[0][2]}) * 0.2f); lds_st<f4>(a + MP, u); lds_st<f4>(a + 2 * MP, c1); lds_st<f4>(a + 3 * MP, sb);
            lds_st<f4>(a + 4 * MP, gb * c1); lds_st<f4>(a + 5 * MP, gb * sb); lds_st<f4>(a + 6 * MP, gb);
            const f2 u2v = lo2(u) + hi2(u), b2v = lo2(b) + hi2(b);
            lds_st<float>(M_US + (wave - 5) * 16 + g * 4, row_sum(fold4(u2v[0], u2v[1], b2v[0], b2v[1])));
        }
        TS(10);
        if (!roleB) { BAR(); ffn_issue(); }
        TS(12);
        BAR();
        modread();
        if (dual) ffn_issue();
        TS(11);
    }
    int e3r = L_E3 + (16 * wave + c) * E3P + g * 32, ko = l < 8 ? WS_KEY + l * 4096 + w * 16 : OOB;
    int sw_ = M_STAT + wave * 16 + g * 4, rid = 2047 - (256 * (l & 7) + s2);
    PIN(e3r); PIN(ko); PIN(sw_); PIN(rid);
    BAR();
    TS(14);

    {
        const u4 d = lds_ld<u4>(e3r); const u2 dh = lds_ld<u2>(e3r + 16);
        SB();
        f4 y[3];
#pragma unroll
        for (int T = 0; T < 3; ++T) y[T] = mfma(u4{dh[0], dh[1], 0, 0}, c3h[T], mfma(d, c3[T], zero4));
#pragma unroll
        for (int T = 0; T < 3; ++T) xc[T] += vga[T] * y[T];
        const f4 t = xc[0] + xc[1] + xc[2];
        f2 q = lo2(xc[0]) * lo2(xc[0]), u = lo2(xc[0]) * lo2(vu[0]);
        q += hi2(xc[0]) * hi2(xc[0]); u += hi2(xc[0]) * hi2(vu[0]);
#pragma unroll
        for (int T = 1; T < 3; ++T) { q += lo2(xc[T]) * lo2(xc[T]); u += lo2(xc[T]) * lo2(vu[T]); q += hi2(xc[T]) * hi2(xc[T]); u += hi2(xc[T]) * hi2(vu[T]); }
        lds_st<float>(sw_, row_sum(fold4(hsum(t), q[0] + q[1], u[0], u[1])));
    }
    if (wave == 0) PRIO(3);
    BAR();
    const f4 st = lds_ld<f4>(M_STAT + (l & 7) * 16);
    const float m2l = st[0] * (1.f / 640.f);
    const float rsl = __builtin_amdgcn_rsqf(fmaxf(st[1] * (1.f / 640.f) - m2l * m2l, 0.f) + 1e-5f);
    const unsigned sbits = as_u(rsl * ((st[2] + st[3]) - m2l * usum) + rconst);
    const unsigned keyl = ((sbits ^ ((unsigned)((int)sbits >> 31) | 0x80000000u)) & 0xfffff800u) | (unsigned)rid;
    unsigned kmax = max(keyl, dpp<0x124>(keyl)); kmax = max(kmax, dpp<0x122>(kmax)); kmax = max(kmax, dpp<0x121>(kmax));
    if (wave == 0) st2<SC1>(u2{kmax, tag}, rw, ko);
    TS(15);
    const unsigned key = rdl(kmax, 0);
    const bool cand = (int)((2047 - (key & 2047)) >> 8) == wave;
    const float m2 = as_f(rdl(as_u(m2l), wave)), rs2 = as_f(rdl(as_u(rsl), wave));
    const rsrc_t ro = mkrs(out, 2048 * 1280);
    u2 ov[3]; f4 z[3];
#pragma unroll
    for (int T = 0; T < 3; ++T) z[T] = xc[T] * rs2 - m2 * rs2;
    if (cand) {
        u2 h[3];
#pragma unroll
        for (int T = 0; T < 3; ++T) h[T] = pkb4(z[T] * lds_ld<f4>(L_MOD + 2 * MP + T * 1024 + l * 16) + lds_ld<f4>(L_MOD + 3 * MP + T * 1024 + l * 16));
        st4<SC1>(u4{h[0][0], h[0][1], h[1][0], tag}, rw, WS_ROW + w * 2048 + l * 16);
        st4<SC1>(u4{h[1][1], h[2][0], h[2][1], tag}, rw, WS_ROW + w * 2048 + 1024 + l * 16);
    }
#pragma unroll
    for (int T = 0; T < 3; ++T) ov[T] = pkb4(xc[T] + (z[T] * vg1[T] + vg0[T]));
    if (!cand) { st2<OUTAUX>(ov[0], ro, ooff, rowb); st2<OUTAUX>(ov[1], ro, 512 + ooff, rowb); st2<OUTAUX>(ov[2], ro, o2, rowb); }
    PIN(ov[0]); PIN(ov[1]); PIN(ov[2]);
    u4 gf[5], uf[5]; u2 df[16];
    if (wave == 0) {
        if (roleF) nap<D_KEY>(); else nap<D_KEYB>();
        const int o = WS_KEY + x8 * 4096 + l * 16;
        u2 k[4];
        TS(16);
#pragma clang loop unroll(disable)
        for (int n = 0; n < SPIN; ++n) {
            FENCE();
#pragma unroll
            for (int i = 0; i < 4; ++i) k[i] = ld2<SC1>(rw, o + i * 1024);
            if (!any((((k[0][1] ^ tag) | (k[1][1] ^ tag)) | ((k[2][1] ^ tag) | (k[3][1] ^ tag))) != 0)) break;
        }
        const unsigned win = wave_umax(max(max(k[0][0], k[1][0]), max(k[2][0], k[3][0])));
        TS(17);
        if (roleF) {
            const int sw = (2047 - (win & 2047)) & 255, ro2 = WS_ROW + ((sw & 31) * 8 + (sw >> 5)) * 2048;
            u4 v0, v1;
#pragma clang loop unroll(disable)
            for (int n = 0; n < SPIN; ++n) {
                FENCE();
                v0 = ld4<SC1>(rw, l * 16, ro2); v1 = ld4<SC1>(rw, l * 16 + 1024, ro2);
                if (!any(((v0[3] ^ tag) | (v1[3] ^ tag)) != 0)) break;
            }
            lds_st<u2>(L_H + ooff, u2{v0[0], v0[1]}); lds_st<u2>(L_H + 512 + ooff, u2{v0[2], v1[0]}); lds_st<u2>(L_H + 1024 + ooff, u2{v1[1], v1[2]});
        }
        lds_st<unsigned>(M_WIN, win);
        TS(18);
        PRIO(0);
    } else if (!lo && roleF) {
        DRAIN();
        const int a = L_GU + wq * 10240 + g * 256 + c * 8, b = L_MT + wq * 8192 + (c >> 2) * 512 + g * 128 + (c & 3) * 8;
#pragma unroll
        for (int s = 0; s < 5; ++s) { gf[s] = lds_tr2(a + s * 1024, a + s * 1024 + 128); uf[s] = lds_tr2(a + 5120 + s * 1024, a + 5120 + s * 1024 + 128); }
#pragma unroll
        for (int i = 0; i < 16; ++i) df[i] = lds_tr(b + (i >> 2) * 2048 + (i & 3) * 32);
        PRIO(2);
    }
    int ha = L_H + wq * 320 + g * 16, pa = M_PART + (g & 1) * 256 + c * 16 + wq * 4;
    PIN(ha); PIN(pa);
    BAR();
    if (!lo && roleF) {
        u4 hf[5];
#pragma unroll
        for (int s = 0; s < 5; ++s) hf[s] = lds_ld<u4>(ha + s * 64);
        SB();
        f4 a = zero4, b = zero4;
#pragma unroll
        for (int s = 0; s < 5; ++s) { a = mfmab(hf[s], gf[s], a); b = mfmab(hf[s], uf[s], b); }
        lds_st<float>(pa, (g & 1) ? b[0] : a[0]);
    }
    const unsigned win = (unsigned)__builtin_amdgcn_readfirstlane((int)lds_ld<unsigned>(M_WIN));
    if (roleF) BAR();
    TS(19);
    const unsigned ytag = (tag >> (8 * (l & 3))) & 0xffu;
    if (wave > 4 && roleF) {
        const float act = silu(hsum(lds_ld<f4>(M_PART + c * 16))) * hsum(lds_ld<f4>(M_PART + 256 + c * 16));
        const unsigned pr = pkb(act, dppf<0x101>(act));
        f4 a = zero4;
#pragma unroll
        for (int i = 0; i < 8; ++i) {
            const b2 s = as_b2(rdl(pr, 2 * i));
#pragma unroll
            for (int j = 0; j < 4; ++j) { const u2 d = df[4 * (i >> 1) + j]; a[j] = __builtin_amdgcn_fdot2_f32_bf16(as_b2(d[i & 1]), s, a[j], false); }
        }
        st4<SC1>(u4{(as_u(a[0]) & ~0xffu) | ytag, as_u(a[1]), as_u(a[2]), as_u(a[3])}, rw, hidx < 160 ? WS_YP + ft * 2560 + hidx * 16 : OOB);
        TS(20);
    }
    if (cand) {
        if (win != key) { st2<OUTAUX>(ov[0], ro, ooff, rowb); st2<OUTAUX>(ov[1], ro, 512 + ooff, rowb); st2<OUTAUX>(ov[2], ro, o2, rowb); }
        else {
#pragma unroll
            for (int T = 0; T < 3; ++T) st4<SC1>(u4{ov[T][0], tag, ov[T][1], tag}, rw, WS_BASE + T * 1024 + 2 * ooff);
        }
    }
    if (wave == 1 && r < 5) {
        const int k = 5 * x8 + r, row = 2047 - (win & 2047), col = 64 * (k >> 2) + 16 * g + 4 * (k & 3) + (l & 3);
        const unsigned kb2 = (win & 0xfffff800u) | 0x400u;
        const float sc = as_f(kb2 ^ ((kb2 >> 31) ? 0x80000000u : 0xffffffffu));
        float gs = lds_ld<float>(L_MOD + 6 * MP + (col >> 8) * 1024 + (k & 3) * 256 + ((col >> 4) & 15) * 16 + (l & 3) * 4) * __builtin_amdgcn_rcpf(1.f + __expf(-sc));
        int yo = WS_YP + (l >> 2) * 2560 + k * 64 + (l & 3) * 16, bo = WS_BASE + (col >> 1) * 8, oo = c < 4 ? row * 1280 + col * 2 : OOB;
        PIN(gs); PIN(yo); PIN(bo); PIN(oo);
        nap<D_YP>();
        u4 v[6]; u2 bs;
        TS(20);
#pragma clang loop unroll(disable)
        for (int n = 0; n < SPIN; ++n) {
            FENCE();
#pragma unroll
            for (int i = 0; i < 6; ++i) v[i] = ld4<SC1>(rw, yo, i * 40960);
            bs = ld2<SC1>(rw, bo);
            unsigned d = 0;
#pragma unroll
            for (int i = 0; i < 6; ++i) d |= v[i][0] ^ ytag;
            if (!any(((d & 0xffu) | (bs[1] ^ tag)) != 0)) break;
        }
        TS(21);
#pragma unroll
        for (int i = 0; i < 6; ++i) v[i][0] &= ~0xffu;
        const f4 a = ((as_f4(v[0]) + as_f4(v[1])) + (as_f4(v[2]) + as_f4(v[3]))) + (as_f4(v[4]) + as_f4(v[5]));
        float t = fold4(a[0], a[1], a[2], a[3]);
        t += dppf<0x124>(t); t += dppf<0x128>(t);
        const float base = (l & 1) ? bfhi(bs[0]) : bflo(bs[0]);
        __builtin_amdgcn_raw_buffer_store_b16((unsigned short)pkb(base + gs * t, 0.f), ro, oo, 0, OUTAUX);
        TS(22);
        asm volatile(".Lgf_end:");
    }
    TSDUMP();
}

extern "C" void kernel_launch(void* const* d_in, const int* in_sizes, int n_in, void* d_out, int out_size, void* d_ws, size_t ws_size, hipStream_t stream) {
    gfnet_mod_kernel<<<256, 512, 0, stream>>>((const char*)d_in[0], (const char*)d_in[9], (const char*)d_in[8], (char*)d_ws, (const char*)d_in[1], (const char*)d_in[2],
                                              (const char*)d_in[4], (char*)d_out, (const char*)d_in[5], (const char*)d_in[6], (const char*)d_in[7]);
}
```
